```python
import torch

def _softmax(x, axis=-1):
    return torch.softmax(x, dim=axis)
def _gelu(x):
    return torch.nn.functional.gelu(x, approximate="tanh")
def _silu(x):
    return torch.nn.functional.silu(x)
def _sigmoid(x):
    return torch.sigmoid(x)
def _layernorm(x, axis=-1, eps=1e-5):
    m = x.mean(dim=axis, keepdim=True)
    v = x.var(dim=axis, keepdim=True, unbiased=False)
    return (x - m) / torch.sqrt(v + eps)
def _scatter_add(tgt, idx, src, axis=0):
    return tgt.clone().index_add_(axis, idx, src)
def _segment_sum(src, seg, n):
    out = torch.zeros((n,) + src.shape[1:], dtype=src.dtype)
    return out.index_add_(0, seg, src)
def _pad(x, pw):
    flat = tuple(v for pair in reversed(pw) for v in pair)
    return torch.nn.functional.pad(x, flat)

kernel_name = 'concat_heads__mha_block_cached__gqa_block_cached__near_ridge'

def setup_inputs():

    _w = (lambda a: (lambda t: t.to(torch.bfloat16) if t.is_floating_point() else t)(torch.as_tensor(a)))
    def _A_setup():
        B, Sq, D = 8, 112, 1024
        H, Sk, Dh = 32, 1024, 128
        return {
            "x": _w((lambda *s: torch.randn(*s, dtype=torch.bfloat16) * 0.8)(B, Sq, D)),
            "k_cache": _w((lambda *s: torch.randn(*s, dtype=torch.bfloat16) * 0.8)(B, H, Sk, Dh)),
            "v_cache": _w((lambda *s: torch.randn(*s, dtype=torch.bfloat16) * 0.05)(B, H, Sk, Dh)),
            "wq": _w((lambda *s: torch.randn(*s, dtype=torch.bfloat16) * 0.8)(D, H * Dh) * (D ** -0.5)),
            "wo": _w((lambda *s: torch.randn(*s, dtype=torch.bfloat16) * 0.05)(H * Dh, D) * ((H * Dh) ** -0.5)),
        }
    _A_in = _A_setup()

    def _B_setup():
        B, Sq, D = 8, 112, 1024
        Hq, Hkv, Sk, Dh = 32, 32, 1024, 128
        assert Hq % Hkv == 0, (Hq, Hkv)
        return {
            "x": _w((lambda *s: torch.randn(*s, dtype=torch.bfloat16) * 0.8)(B, Sq, D)),
            "k_cache": _w((lambda *s: torch.randn(*s, dtype=torch.bfloat16) * 0.8)(B, Hkv, Sk, Dh)),
            "v_cache": _w((lambda *s: torch.randn(*s, dtype=torch.bfloat16) * 0.05)(B, Hkv, Sk, Dh)),
            "wq": _w((lambda *s: torch.randn(*s, dtype=torch.bfloat16) * 0.8)(D, Hq * Dh) * (D ** -0.5)),
            "wo": _w((lambda *s: torch.randn(*s, dtype=torch.bfloat16) * 0.05)(Hq * Dh, D) * ((Hq * Dh) ** -0.5)),
        }
    _B_in = _B_setup()

    return {
        "A_x": _A_in["x"],
        "A_k_cache": _A_in["k_cache"],
        "A_v_cache": _A_in["v_cache"],
        "A_wq": _A_in["wq"],
        "A_wo": _A_in["wo"],
        "B_x": _B_in["x"],
        "B_k_cache": _B_in["k_cache"],
        "B_v_cache": _B_in["v_cache"],
        "B_wq": _B_in["wq"],
        "B_wo": _B_in["wo"],
    }

def reference(A_x, A_k_cache, A_v_cache, A_wq, A_wo, B_x, B_k_cache, B_v_cache, B_wq, B_wo):
    def _A_ref(x, k_cache, v_cache, wq, wo):
        B, Sq, D = x.shape
        H, Dh = 32, 128
        q = (lambda x, *d: x.permute(*d))((x @ wq).reshape(B, Sq, H, Dh), 0, 2, 1, 3)
        scores = torch.einsum("bhqd,bhkd->bhqk", q, k_cache) * (Dh ** -0.5)
        p = _softmax(scores, axis=-1)
        o = torch.einsum("bhqk,bhkd->bhqd", p, v_cache)
        return (lambda x, *d: x.permute(*d))(o, 0, 2, 1, 3).reshape(B, Sq, H * Dh) @ wo

    def _B_ref(x, k_cache, v_cache, wq, wo):
        B, Sq, D = x.shape
        Hq, Dh = 32, 128
        Hkv = k_cache.shape[1]
        rep = Hq // Hkv
        q = (lambda x, *d: x.permute(*d))((x @ wq).reshape(B, Sq, Hq, Dh), 0, 2, 1, 3)
        k = (lambda x, n, axis: torch.repeat_interleave(x, n, dim=axis))(k_cache, rep, axis=1)
        v = (lambda x, n, axis: torch.repeat_interleave(x, n, dim=axis))(v_cache, rep, axis=1)
        scores = torch.einsum("bhqd,bhkd->bhqk", q, k) * (Dh ** -0.5)
        p = _softmax(scores, axis=-1)
        o = torch.einsum("bhqk,bhkd->bhqd", p, v)
        return (lambda x, *d: x.permute(*d))(o, 0, 2, 1, 3).reshape(B, Sq, Hq * Dh) @ wo

    a = _A_ref(A_x, A_k_cache, A_v_cache, A_wq, A_wo)
    b = _B_ref(B_x, B_k_cache, B_v_cache, B_wq, B_wo)
    return torch.concatenate([a, b], axis=-1)
```

```cpp
#include <hip/hip_runtime.h>
#include <hip/hip_bf16.h>
typedef unsigned short u16;
__device__ __forceinline__ float b2f(u16 x){ return __uint_as_float(((unsigned)x)<<16); }
__device__ __forceinline__ u16 f2b(float f){ unsigned u=__float_as_uint(f); return (u16)((u+0x7FFF+((u>>16)&1))>>16); }
constexpr int B=8,SQ=112,D=1024,H=32,SK=1024,DH=128;
__global__ void gemm_naive(const u16* A,const u16* W,u16* C,int M,int N,int K,int ldc){
  __shared__ float as[16][17], ws[16][17];
  int tx=threadIdx.x&15, ty=threadIdx.x>>4;
  int row=blockIdx.y*16+ty, col=blockIdx.x*16+tx;
  float acc=0;
  for(int k0=0;k0<K;k0+=16){
    as[ty][tx]=b2f(A[(size_t)row*K+k0+tx]);
    ws[ty][tx]=b2f(W[(size_t)(k0+ty)*N+col]);
    __syncthreads();
    for(int k=0;k<16;k++) acc+=as[ty][k]*ws[k][tx];
    __syncthreads();
  }
  C[(size_t)row*ldc+col]=f2b(acc);
}
__global__ void attn_naive(const u16* Q,const u16* Kc,const u16* Vc,u16* O){
  int q=blockIdx.x, h=blockIdx.y, b=blockIdx.z; int t=threadIdx.x;
  __shared__ float qs[DH], s[SK], red[128];
  qs[t]=b2f(Q[((size_t)(b*SQ+q))*H*DH+h*DH+t]);
  __syncthreads();
  const u16* Kp=Kc+((size_t)(b*H+h))*SK*DH; const u16* Vp=Vc+((size_t)(b*H+h))*SK*DH;
  float mx=-1e30f;
  for(int k=t;k<SK;k+=128){ float a=0; for(int d=0;d<DH;d++) a+=qs[d]*b2f(Kp[(size_t)k*DH+d]); a*=0.08838834764831845f; s[k]=a; mx=fmaxf(mx,a);}
  red[t]=mx; __syncthreads();
  for(int o=64;o>0;o>>=1){ if(t<o) red[t]=fmaxf(red[t],red[t+o]); __syncthreads(); }
  mx=red[0]; __syncthreads();
  float sm=0; for(int k=t;k<SK;k+=128){ float e=__expf(s[k]-mx); s[k]=e; sm+=e; }
  red[t]=sm; __syncthreads();
  for(int o=64;o>0;o>>=1){ if(t<o) red[t]+=red[t+o]; __syncthreads(); }
  sm=red[0];
  float acc=0; for(int k=0;k<SK;k++) acc+=s[k]*b2f(Vp[(size_t)k*DH+t]);
  O[((size_t)(b*SQ+q))*H*DH+h*DH+t]=f2b(acc/sm);
}
extern "C" void kernel_launch(void* const* d_in, const int* in_sizes, int n_in, void* d_out, int out_size, void* d_ws, size_t ws_size, hipStream_t stream){
  u16* ws=(u16*)d_ws;
  for(int br=0;br<2;br++){
    const u16* x=(const u16*)d_in[br*5+0]; const u16* k=(const u16*)d_in[br*5+1]; const u16* v=(const u16*)d_in[br*5+2];
    const u16* wq=(const u16*)d_in[br*5+3]; const u16* wo=(const u16*)d_in[br*5+4];
    u16* Q=ws+(size_t)br*2*B*SQ*H*DH; u16* O=Q+(size_t)B*SQ*H*DH;
    gemm_naive<<<dim3(H*DH/16,B*SQ/16),256,0,stream>>>(x,wq,Q,B*SQ,H*DH,D,H*DH);
    attn_naive<<<dim3(SQ,H,B),128,0,stream>>>(Q,k,v,O);
    gemm_naive<<<dim3(D/16,B*SQ/16),256,0,stream>>>(O,wo,(u16*)d_out+br*D,B*SQ,D,H*DH,2*D);
  }
}
```

```cpp
#include <hip/hip_runtime.h>
#include <hip/hip_bf16.h>
typedef __bf16 bf16x8 __attribute__((ext_vector_type(8)));
typedef __bf16 bf16x4 __attribute__((ext_vector_type(4)));
typedef __bf16 bf16x2 __attribute__((ext_vector_type(2)));
typedef float f32x4 __attribute__((ext_vector_type(4)));
typedef float f32x2 __attribute__((ext_vector_type(2)));
typedef unsigned u32x4 __attribute__((ext_vector_type(4)));
typedef unsigned u32x2 __attribute__((ext_vector_type(2)));
#define DI __device__ __forceinline__
#define AS3 __attribute__((address_space(3)))
#define KV_NT 1
#define W_NT 1
constexpr int SQ = 112, DM = 1024, NH = 32, SK = 1024, DH = 128;
constexpr unsigned REG = 16384u;
constexpr float QSCALE = 0.12751743082f;
constexpr float THR = 8.0f;

DI unsigned rfl(unsigned x) { return __builtin_amdgcn_readfirstlane(x); }
DI bf16x8 lds128(unsigned a) { return *(AS3 bf16x8*)a; }
DI bf16x4 ldstr(unsigned a) { return __builtin_amdgcn_ds_read_tr16_b64_v4bf16((AS3 bf16x4*)a); }
DI bf16x8 cat(bf16x4 a, bf16x4 b) { return __builtin_shufflevector(a, b, 0, 1, 2, 3, 4, 5, 6, 7); }
DI unsigned cvtpk(float lo, float hi) { f32x2 v = {lo, hi}; bf16x2 b = __builtin_convertvector(v, bf16x2); return __builtin_bit_cast(unsigned, b); }
DI f32x4 mfma(bf16x8 a, bf16x8 b, f32x4 c) { return __builtin_amdgcn_mfma_f32_16x16x32_bf16(a, b, c, 0, 0, 0); }
typedef __amdgpu_buffer_rsrc_t srd_t;
DI srd_t mksrd(const void* p, unsigned bytes) { return __builtin_amdgcn_make_buffer_rsrc((void*)p, (short)0, (int)bytes, 0x00020000); }
template <int NT> DI void dma(unsigned m0, unsigned voff, srd_t srd, unsigned so) {
  if constexpr (NT) asm volatile("s_mov_b32 m0, %0\n\ts_nop 0\n\tbuffer_load_dwordx4 %1, %2, %3 offen nt lds" ::"s"(m0), "v"(voff), "s"(srd), "s"(so) : "memory");
  else              asm volatile("s_mov_b32 m0, %0\n\ts_nop 0\n\tbuffer_load_dwordx4 %1, %2, %3 offen lds" ::"s"(m0), "v"(voff), "s"(srd), "s"(so) : "memory");
}
template <int N> DI void wait_vm() { asm volatile("s_waitcnt vmcnt(%0)" ::"i"(N) : "memory"); }
DI void bar() { asm volatile("s_waitcnt lgkmcnt(0)\n\ts_barrier" ::: "memory"); }
DI float max3f(float a, float b, float c) { float r; asm("v_max3_f32 %0, %1, %2, %3" : "=v"(r) : "v"(a), "v"(b), "v"(c)); return r; }

struct Lane { unsigned l, i, g, qp, p; };

template <int NQ> DI void qproj_step(f32x4 (&acc)[2][NQ], unsigned wa0, unsigned wa1, unsigned xa0, unsigned xa1) {
#pragma unroll
  for (int ks = 0; ks < 2; ++ks) {
    const bf16x8 a0 = cat(ldstr(wa0 + 8192 * ks), ldstr(wa0 + 8192 * ks + 1024));
    const bf16x8 a1 = cat(ldstr(wa1 + 8192 * ks), ldstr(wa1 + 8192 * ks + 1024));
#pragma unroll
    for (int qt = 0; qt < NQ; ++qt) {
      const bf16x8 b = lds128((ks ? xa1 : xa0) + 2048 * qt);
      acc[0][qt] = mfma(a0, b, acc[0][qt]);
      acc[1][qt] = mfma(a1, b, acc[1][qt]);
    }
  }
}
template <int NQ> DI void q_write(const f32x4 (&acc)[2][NQ], unsigned qbuf, const Lane& L, unsigned s, unsigned q0) {
#pragma unroll
  for (int u = 0; u < 2; ++u) {
    const unsigned a = qbuf + 256u * (16u * q0 + L.i) + 16u * ((2u * (2u * s + u) + (L.g >> 1)) ^ L.i) + 8u * (L.g & 1u);
#pragma unroll
    for (int qt = 0; qt < NQ; ++qt) {
      u32x2 w = {cvtpk(acc[u][qt][0] * QSCALE, acc[u][qt][1] * QSCALE), cvtpk(acc[u][qt][2] * QSCALE, acc[u][qt][3] * QSCALE)};
      *(AS3 u32x2*)(a + 4096u * qt) = w;
    }
  }
}

struct Src { srd_t x, w0, w1, k0, v0, k1, v1; };

template <bool LOADER> DI void phase0(const Src& S, const Lane& L, unsigned wid, unsigned lds, unsigned vk, unsigned vv) {
  constexpr int NQ = LOADER ? 3 : 4;
  const unsigned s = wid & 3u, q0 = LOADER ? 4u : 0u;
  const unsigned lr = L.l >> 4, lc = L.l & 15u;
  const unsigned vw = 8192u * lr + 16u * (lc ^ (2u * (lr | ((wid & 1u) << 2))));
  const unsigned vx0 = 2048u * (L.l >> 3) + 16u * ((L.l & 7u) ^ lr) + 32768u * wid, vx1 = (vx0 ^ 64u) + 16384u;
  const unsigned h = L.qp | ((L.g & 1u) << 2);
  const unsigned wa0 = 256u * (8u * L.g + L.qp) + 16u * ((2u * (2u * s) + (L.p >> 1)) ^ (2u * h)) + 8u * (L.p & 1u), wa1 = wa0 ^ 32u;
  const unsigned xa0 = 128u * L.i + 16u * (L.g ^ (L.i >> 1)) + 2048u * q0, xa1 = xa0 ^ 64u;
  auto issue_g = [&](int i) {
    const unsigned st = lds + (unsigned)((i + 2) & 3) * 2u * REG;
    dma<0>(st + 2048u * wid, vx0, S.x, 128u * i);
    dma<0>(st + 2048u * wid + 1024u, vx1, S.x, 128u * i);
    dma<W_NT>(st + REG + 2048u * wid, vw, S.w0, 524288u * i + 65536u * wid);
    dma<W_NT>(st + REG + 2048u * wid + 1024u, vw, S.w0, 524288u * i + 65536u * wid + 32768u);
  };
  auto issue_kv = [&](int t, unsigned slot) {
    const unsigned w = wid & 3u;
#pragma unroll
    for (int q = 0; q < 4; ++q) dma<KV_NT>(slot + 1024u * w + 4096u * q, vk, S.k0, 16384u * t + 1024u * w + 4096u * q);
#pragma unroll
    for (int q = 0; q < 4; ++q) dma<KV_NT>(slot + REG + 1024u * w + 4096u * q, vv, S.v0, 16384u * t + 1024u * w + 4096u * q);
  };
  f32x4 acc[2][NQ];
#pragma unroll
  for (int u = 0; u < 2; ++u)
#pragma unroll
    for (int qt = 0; qt < NQ; ++qt) acc[u][qt] = f32x4{0.f, 0.f, 0.f, 0.f};
  issue_g(0); issue_g(1); issue_g(2);
  if constexpr (LOADER) issue_kv(0, lds + 8u * REG);
#pragma unroll
  for (int i = 0; i < 16; ++i) {
    if (i <= 2) { if constexpr (LOADER) wait_vm<16>(); else wait_vm<8>(); }
    else if (i <= 13) wait_vm<8>();
    else if (i == 14) { if constexpr (LOADER) wait_vm<12>(); else wait_vm<4>(); }
    else { if constexpr (LOADER) wait_vm<16>(); else wait_vm<0>(); }
    bar();
    if (i + 3 <= 15) issue_g(i + 3);
    if constexpr (LOADER) { if (i == 13) issue_kv(1, lds + 4u * REG); if (i == 14) issue_kv(2, lds + 6u * REG); }
    const unsigned st = lds + (unsigned)((i + 2) & 3) * 2u * REG;
    qproj_step<NQ>(acc, st + REG + wa0, st + REG + wa1, st + xa0, st + xa1);
  }
  q_write<NQ>(acc, lds, L, s, q0);
}

template <int NQ> DI void attn_item(const Lane& L, unsigned lds, unsigned qbuf, unsigned wid, unsigned& slot, char* __restrict__ odst) {
  unsigned ka[4], va[8];
#pragma unroll
  for (int s = 0; s < 4; ++s) ka[s] = 256u * L.i + 16u * ((4u * s + L.g) ^ L.i);
  const unsigned hv = 4u * (L.g & 1u) + L.qp;
#pragma unroll
  for (int dt = 0; dt < 8; ++dt) va[dt] = REG + 256u * (4u * L.g + L.qp) + 32u * (dt ^ hv) + 16u * (L.p >> 1) + 8u * (L.p & 1u);
  bf16x8 Q[NQ][4];
  f32x4 O[8][NQ], Ls[NQ], negm[NQ];
#pragma unroll
  for (int qt = 0; qt < NQ; ++qt) {
    Ls[qt] = f32x4{0.f, 0.f, 0.f, 0.f}; negm[qt] = f32x4{0.f, 0.f, 0.f, 0.f};
#pragma unroll
    for (int dt = 0; dt < 8; ++dt) O[dt][qt] = f32x4{0.f, 0.f, 0.f, 0.f};
  }
  const u32x4 onesu = {0x3F803F80u, 0x3F803F80u, 0x3F803F80u, 0x3F803F80u};
  const bf16x8 ones = __builtin_bit_cast(bf16x8, onesu);
  for (int t = 0; t < 16; ++t) {
    bar();
    if (t == 0) {
#pragma unroll
      for (int qt = 0; qt < NQ; ++qt)
#pragma unroll
        for (int s = 0; s < 4; ++s) Q[qt][s] = lds128(qbuf + ka[s] + 4096u * (2u * wid + qt));
    }
    f32x4 S[4][NQ];
#pragma unroll
    for (int s = 0; s < 4; ++s)
#pragma unroll
      for (int kt = 0; kt < 4; ++kt) {
        const bf16x8 a = lds128(slot + ka[s] + 4096u * kt);
#pragma unroll
        for (int qt = 0; qt < NQ; ++qt) S[kt][qt] = mfma(a, Q[qt][s], s ? S[kt][qt] : negm[qt]);
      }
    float mx[NQ];
    bool grow = t == 0;
#pragma unroll
    for (int qt = 0; qt < NQ; ++qt) {
      float m0 = max3f(S[0][qt][0], S[0][qt][1], S[0][qt][2]), m1 = max3f(S[0][qt][3], S[1][qt][0], S[1][qt][1]);
      m0 = max3f(m0, S[1][qt][2], S[1][qt][3]); m1 = max3f(m1, S[2][qt][0], S[2][qt][1]);
      m0 = max3f(m0, S[2][qt][2], S[2][qt][3]); m1 = max3f(m1, S[3][qt][0], S[3][qt][1]);
      m0 = max3f(m0, S[3][qt][2], S[3][qt][3]);
      mx[qt] = fmaxf(m0, m1);
      grow |= mx[qt] > THR;
    }
    if (__builtin_expect(__any((int)grow), 0)) {
#pragma unroll
      for (int qt = 0; qt < NQ; ++qt) {
        float m = fmaxf(mx[qt], __shfl_xor(mx[qt], 16)); m = fmaxf(m, __shfl_xor(m, 32));
        const float dl = t == 0 ? m : fmaxf(m, 0.f), f = t == 0 ? 1.f : __builtin_amdgcn_exp2f(-dl);
#pragma unroll
        for (int kt = 0; kt < 4; ++kt) S[kt][qt] -= dl;
        negm[qt] -= dl; Ls[qt] *= f;
#pragma unroll
        for (int dt = 0; dt < 8; ++dt) O[dt][qt] *= f;
      }
    }
    bf16x8 P[2][NQ];
#pragma unroll
    for (int qt = 0; qt < NQ; ++qt)
#pragma unroll
      for (int ks = 0; ks < 2; ++ks) {
        u32x4 w;
#pragma unroll
        for (int e = 0; e < 4; ++e) {
          const f32x4 sv = S[2 * ks + (e >> 1)][qt];
          w[e] = cvtpk(__builtin_amdgcn_exp2f(sv[2 * (e & 1)]), __builtin_amdgcn_exp2f(sv[2 * (e & 1) + 1]));
        }
        P[ks][qt] = __builtin_bit_cast(bf16x8, w);
      }
#pragma unroll
    for (int ks = 0; ks < 2; ++ks) {
#pragma unroll
      for (int dt = 0; dt < 8; ++dt) {
        const bf16x8 a = cat(ldstr(slot + va[dt] + 8192u * ks), ldstr(slot + va[dt] + 8192u * ks + 4096u));
#pragma unroll
        for (int qt = 0; qt < NQ; ++qt) O[dt][qt] = mfma(a, P[ks][qt], O[dt][qt]);
      }
#pragma unroll
      for (int qt = 0; qt < NQ; ++qt) Ls[qt] = mfma(ones, P[ks][qt], Ls[qt]);
    }
    slot = slot == lds + 8u * REG ? lds + 4u * REG : slot + 2u * REG;
  }
#pragma unroll
  for (int qt = 0; qt < NQ; ++qt) {
    const float inv = 1.0f / Ls[qt][0];
#pragma unroll
    for (int s = 0; s < 4; ++s) {
      const f32x4 a = O[2 * s][qt] * inv, b = O[2 * s + 1][qt] * inv;
      const u32x4 w = {cvtpk(a[0], a[1]), cvtpk(a[2], a[3]), cvtpk(b[0], b[1]), cvtpk(b[2], b[3])};
      *(u32x4*)(odst + (s * 7 + 2 * wid + qt) * 1024 + L.l * 16) = w;
    }
  }
}

DI void loader_role(const Src& S, const Lane& L, unsigned w, unsigned lds, unsigned vk, unsigned vv) {
  const unsigned lr = L.l >> 4, lc = L.l & 15u;
  const unsigned vw0 = 8192u * lr + 16u * (lc ^ (2u * lr)), vw1 = vw0 ^ 128u;
  const unsigned vx0 = 2048u * (L.l >> 3) + 16u * ((L.l & 7u) ^ lr) + 65536u * w;
  const unsigned vx[4] = {vx0, (vx0 ^ 64u) + 16384u, vx0 + 32768u, (vx0 ^ 64u) + 49152u};
  const unsigned h = L.qp | ((L.g & 1u) << 2);
  const unsigned wa0 = 256u * (8u * L.g + L.qp) + 16u * ((2u * (2u * w) + (L.p >> 1)) ^ (2u * h)) + 8u * (L.p & 1u), wa1 = wa0 ^ 32u;
  const unsigned xa0 = 128u * L.i + 16u * (L.g ^ (L.i >> 1)), xa1 = xa0 ^ 64u;
  auto issue_g = [&](unsigned i, unsigned st) {
#pragma unroll
    for (int q = 0; q < 4; ++q) dma<0>(st + 4096u * w + 1024u * q, vx[q], S.x, 128u * i);
#pragma unroll
    for (int q = 0; q < 4; ++q) dma<W_NT>(st + REG + 4096u * w + 1024u * q, (q & 2) ? vw1 : vw0, S.w1, 524288u * i + 131072u * w + 32768u * q);
  };
  auto issue_kv = [&](srd_t k, srd_t v, unsigned t, unsigned slot) {
#pragma unroll
    for (int q = 0; q < 4; ++q) dma<KV_NT>(slot + 1024u * w + 4096u * q, vk, k, 16384u * t + 1024u * w + 4096u * q);
#pragma unroll
    for (int q = 0; q < 4; ++q) dma<KV_NT>(slot + REG + 1024u * w + 4096u * q, vv, v, 16384u * t + 1024u * w + 4096u * q);
  };
  f32x4 acc[2][7];
#pragma unroll
  for (int u = 0; u < 2; ++u)
#pragma unroll
    for (int qt = 0; qt < 7; ++qt) acc[u][qt] = f32x4{0.f, 0.f, 0.f, 0.f};
  unsigned slot = lds + 8u * REG;
  bar();
  issue_g(0, lds + 2u * REG);
  wait_vm<0>();
  for (unsigned t = 1; t < 16; ++t) {
    bar();
    const unsigned stn = lds + ((t + 1) & 1u) * 2u * REG, stc = lds + (t & 1u) * 2u * REG;
    issue_g(t, stn);
    if (t + 2 < 16) issue_kv(S.k0, S.v0, t + 2, slot); else issue_kv(S.k1, S.v1, t + 2 - 16, slot);
    slot = slot == lds + 8u * REG ? lds + 4u * REG : slot + 2u * REG;
    qproj_step<7>(acc, stc + REG + wa0, stc + REG + wa1, stc + xa0, stc + xa1);
    wait_vm<8>();
  }
  bar();
  issue_kv(S.k1, S.v1, 2, slot);
  slot = slot == lds + 8u * REG ? lds + 4u * REG : slot + 2u * REG;
  qproj_step<7>(acc, lds + REG + wa0, lds + REG + wa1, lds + xa0, lds + xa1);
  q_write<7>(acc, lds + 2u * REG, L, w, 0);
  bar();
  wait_vm<8>();
  for (unsigned t = 1; t < 16; ++t) {
    bar();
    if (t + 2 < 16) { issue_kv(S.k1, S.v1, t + 2, slot); wait_vm<8>(); } else wait_vm<0>();
    slot = slot == lds + 8u * REG ? lds + 4u * REG : slot + 2u * REG;
  }
}

__global__ __launch_bounds__(512, 2) void qattn_kernel(const char* xA, const char* kA, const char* vA, const char* wqA,
                                                       const char* xB, const char* kB, const char* vB, const char* wqB, char* ws) {
  extern __shared__ __attribute__((aligned(16))) char smem[];
  const unsigned lds = (unsigned)(size_t)(AS3 char*)smem;
  const unsigned wid = rfl(threadIdx.x >> 6);
  Lane L; L.l = threadIdx.x & 63u; L.i = L.l & 15u; L.g = L.l >> 4; L.qp = L.i >> 2; L.p = L.i & 3u;
  const unsigned xcd = blockIdx.x & 7u, sl = blockIdx.x >> 3, br = xcd >> 2, j = (xcd & 3u) * 4u + (sl & 3u), b = sl >> 2;
  const char* x = br ? xB : xA; const char* kc = br ? kB : kA; const char* vc = br ? vB : vA; const char* wq = br ? wqB : wqA;
  Src S;
  S.x = mksrd(x + (size_t)b * SQ * DM * 2, SQ * DM * 2);
  S.w0 = mksrd(wq + j * 256u, DM * NH * DH * 2 - j * 256u);
  S.w1 = mksrd(wq + (16u + j) * 256u, DM * NH * DH * 2 - (16u + j) * 256u);
  const size_t it0 = (size_t)(b * NH + j) * SK * DH * 2, it1 = it0 + (size_t)16 * SK * DH * 2;
  S.k0 = mksrd(kc + it0, SK * DH * 2); S.v0 = mksrd(vc + it0, SK * DH * 2);
  S.k1 = mksrd(kc + it1, SK * DH * 2); S.v1 = mksrd(vc + it1, SK * DH * 2);
  const unsigned w4 = wid & 3u, lr = L.l >> 4, lc = L.l & 15u;
  const unsigned vk = 256u * lr + 16u * (lc ^ (4u * w4 + lr));
  const unsigned vv = 256u * lr + 16u * (lc ^ (2u * (4u * (w4 & 1u) + lr)));
  char* of = ws + (size_t)((br * 8u + b) * NH + j) * 28672u;
  if (wid >= 4) {
    phase0<true>(S, L, wid, lds, vk, vv);
    loader_role(S, L, w4, lds, vk, vv);
  } else {
    phase0<false>(S, L, wid, lds, vk, vv);
    unsigned slot = lds + 8u * REG;
    if (wid < 3) {
      attn_item<2>(L, lds, lds, wid, slot, of);
      bar();
      attn_item<2>(L, lds, lds + 2u * REG, wid, slot, of + 16u * 28672u);
    } else {
      attn_item<1>(L, lds, lds, wid, slot, of);
      bar();
      attn_item<1>(L, lds, lds + 2u * REG, wid, slot, of + 16u * 28672u);
    }
  }
}

__global__ __launch_bounds__(512, 2) void oproj_kernel(const char* woA, const char* woB, const char* ws, char* out) {
  extern __shared__ __attribute__((aligned(16))) char smem[];
  const unsigned lds = (unsigned)(size_t)(AS3 char*)smem;
  const unsigned wid = rfl(threadIdx.x >> 6), l = threadIdx.x & 63u, i = l & 15u, g = l >> 4, qp = i >> 2, p = i & 3u;
  const unsigned xcd = blockIdx.x & 7u, sl = blockIdx.x >> 3, br = xcd >> 2, j = (xcd & 3u) * 4u + (sl & 3u), b = sl >> 2;
  const char* wo = br ? woB : woA;
  const srd_t sw = mksrd(wo + j * 128u, NH * DH * DM * 2 - j * 128u);
  const char* of = ws + (size_t)((br * 8u + b) * NH + 4u * wid) * 28672u + l * 16u;
  const unsigned my = lds + wid * 16384u;
  const unsigned vw = 2048u * (l >> 3) + 16u * ((l & 7u) ^ (2u * ((l >> 4) & 3u)));
  unsigned wa[4];
#pragma unroll
  for (int nt = 0; nt < 4; ++nt) wa[nt] = my + 128u * (4u * g + qp) + 32u * (nt ^ (((4u * g + qp) >> 1) & 3u)) + 16u * (p >> 1) + 8u * (p & 1u);
  f32x4 acc[4][7];
#pragma unroll
  for (int nt = 0; nt < 4; ++nt)
#pragma unroll
    for (int qt = 0; qt < 7; ++qt) acc[nt][qt] = f32x4{0.f, 0.f, 0.f, 0.f};
  auto issue_w = [&](int k) {
#pragma unroll
    for (int n = 0; n < 4; ++n) dma<W_NT>(my + (k & 3) * 4096u + 1024u * n, vw, sw, (wid * 512u + k * 32u + 8u * n) * 2048u);
  };
  bf16x8 Bf[3][7];
  auto issue_o = [&](int k) {
#pragma unroll
    for (int qt = 0; qt < 7; ++qt)
      asm volatile("global_load_dwordx4 %0, %1, off offset:%2" : "=v"(Bf[k % 3][qt]) : "v"(of + k * 7168 + (qt >> 2) * 4096), "i"((qt & 3) * 1024) : "memory");
  };
  issue_w(0); issue_o(0); issue_w(1); issue_o(1);
#pragma unroll
  for (int k = 0; k < 16; ++k) {
    if (k + 2 < 16) { issue_w(k + 2); issue_o(k + 2); }
    if (k + 2 < 16) asm volatile("s_waitcnt vmcnt(22)" : "+v"(Bf[k % 3][0]), "+v"(Bf[k % 3][1]), "+v"(Bf[k % 3][2]), "+v"(Bf[k % 3][3]), "+v"(Bf[k % 3][4]), "+v"(Bf[k % 3][5]), "+v"(Bf[k % 3][6])::"memory");
    else if (k + 1 < 16) asm volatile("s_waitcnt vmcnt(11)" : "+v"(Bf[k % 3][0]), "+v"(Bf[k % 3][1]), "+v"(Bf[k % 3][2]), "+v"(Bf[k % 3][3]), "+v"(Bf[k % 3][4]), "+v"(Bf[k % 3][5]), "+v"(Bf[k % 3][6])::"memory");
    else asm volatile("s_waitcnt vmcnt(0)" : "+v"(Bf[k % 3][0]), "+v"(Bf[k % 3][1]), "+v"(Bf[k % 3][2]), "+v"(Bf[k % 3][3]), "+v"(Bf[k % 3][4]), "+v"(Bf[k % 3][5]), "+v"(Bf[k % 3][6])::"memory");
#pragma unroll
    for (int nt = 0; nt < 4; ++nt) {
      const bf16x8 a = cat(ldstr(wa[nt] + (k & 3) * 4096u), ldstr(wa[nt] + (k & 3) * 4096u + 2048u));
#pragma unroll
      for (int qt = 0; qt < 7; ++qt) acc[nt][qt] = mfma(a, Bf[k % 3][qt], acc[nt][qt]);
    }
    asm volatile("s_waitcnt lgkmcnt(0)" ::: "memory");
  }
  bar();
#pragma unroll
  for (int hlf = 0; hlf < 2; ++hlf) {
#pragma unroll
    for (int e = 0; e < 14; ++e) *(AS3 f32x4*)(lds + (wid * 14u + e) * 1024u + l * 16u) = acc[2 * hlf + e / 7][e % 7];
    bar();
#pragma unroll
    for (int rep = 0; rep < 2; ++rep) {
      const unsigned e = wid + 8u * rep;
      if (e < 14u) {
        f32x4 sum = *(AS3 f32x4*)(lds + e * 1024u + l * 16u);
#pragma unroll
        for (int w = 1; w < 8; ++w) sum += *(AS3 f32x4*)(lds + (w * 14u + e) * 1024u + l * 16u);
        const unsigned nt = 2u * hlf + (e >= 7u), qt = e >= 7u ? e - 7u : e;
        const u32x2 o = {cvtpk(sum[0], sum[1]), cvtpk(sum[2], sum[3])};
        *(u32x2*)(out + (size_t)(b * SQ + 16u * qt + i) * 4096u + br * 2048u + j * 128u + nt * 32u + g * 8u) = o;
      }
    }
    bar();
  }
}

extern "C" void kernel_launch(void* const* d_in, const int* in_sizes, int n_in, void* d_out, int out_size, void* d_ws, size_t ws_size, hipStream_t stream) {
  (void)hipFuncSetAttribute((const void*)qattn_kernel, hipFuncAttributeMaxDynamicSharedMemorySize, 163840);
  (void)hipFuncSetAttribute((const void*)oproj_kernel, hipFuncAttributeMaxDynamicSharedMemorySize, 131072);
  qattn_kernel<<<256, 512, 163840, stream>>>((const char*)d_in[0], (const char*)d_in[1], (const char*)d_in[2], (const char*)d_in[3],
                                             (const char*)d_in[5], (const char*)d_in[6], (const char*)d_in[7], (const char*)d_in[8], (char*)d_ws);
  oproj_kernel<<<256, 512, 131072, stream>>>((const char*)d_in[4], (const char*)d_in[9], (const char*)d_ws, (char*)d_out);
}
```

```cpp
#include <hip/hip_runtime.h>
#include <hip/hip_bf16.h>
#include <utility>
typedef __bf16 bf16x8 __attribute__((ext_vector_type(8)));
typedef __bf16 bf16x4 __attribute__((ext_vector_type(4)));
typedef __bf16 bf16x2 __attribute__((ext_vector_type(2)));
typedef float f32x4 __attribute__((ext_vector_type(4)));
typedef float f32x2 __attribute__((ext_vector_type(2)));
typedef unsigned u32x4 __attribute__((ext_vector_type(4)));
typedef unsigned u32x2 __attribute__((ext_vector_type(2)));
#define DI __device__ __forceinline__
#define AS3 __attribute__((address_space(3)))
#define KV_NT 1
#define W_NT 1
constexpr int SQ = 112, DM = 1024, NH = 32, SK = 1024, DH = 128;
constexpr unsigned REG = 16384u;
constexpr float QSCALE = 0.12751743082f;
constexpr float THR = 8.0f;
constexpr int NSTAMP = 8;

DI unsigned rfl(unsigned x) { return __builtin_amdgcn_readfirstlane(x); }
DI bf16x8 lds128(unsigned a) { return *(AS3 bf16x8*)a; }
DI bf16x4 ldstr(unsigned a) { return __builtin_amdgcn_ds_read_tr16_b64_v4bf16((AS3 bf16x4*)a); }
DI bf16x8 cat(bf16x4 a, bf16x4 b) { return __builtin_shufflevector(a, b, 0, 1, 2, 3, 4, 5, 6, 7); }
DI unsigned cvtpk(float lo, float hi) { f32x2 v = {lo, hi}; bf16x2 b = __builtin_convertvector(v, bf16x2); return __builtin_bit_cast(unsigned, b); }
DI f32x4 mfma(bf16x8 a, bf16x8 b, f32x4 c) { return __builtin_amdgcn_mfma_f32_16x16x32_bf16(a, b, c, 0, 0, 0); }
typedef __amdgpu_buffer_rsrc_t srd_t;
DI srd_t mksrd(const void* p, unsigned bytes) { return __builtin_amdgcn_make_buffer_rsrc((void*)p, (short)0, (int)bytes, 0x00020000); }
template <int NT> DI void dma(unsigned m0, unsigned voff, srd_t srd, unsigned so) {
  if constexpr (NT) asm volatile("s_mov_b32 m0, %0\n\ts_nop 0\n\tbuffer_load_dwordx4 %1, %2, %3 offen nt lds" ::"s"(m0), "v"(voff), "s"(srd), "s"(so) : "memory");
  else              asm volatile("s_mov_b32 m0, %0\n\ts_nop 0\n\tbuffer_load_dwordx4 %1, %2, %3 offen lds" ::"s"(m0), "v"(voff), "s"(srd), "s"(so) : "memory");
}
template <int N> DI void wait_vm() { asm volatile("s_waitcnt vmcnt(%0)" ::"i"(N) : "memory"); }
DI void bar() { asm volatile("s_waitcnt lgkmcnt(0)\n\ts_barrier" ::: "memory"); }
DI float max2f(float a, float b) { return __builtin_elementwise_maximum(a, b); }
DI float max3f(float a, float b, float c) { return max2f(max2f(a, b), c); }
#define STAMP(k, i)

struct Lane { unsigned l, i, g, qp, p; };
template <int N> struct IC { static constexpr int v = N; };
template <class F, int... Is> DI void sfor_(F&& f, std::integer_sequence<int, Is...>) { (f(IC<Is>{}), ...); }
template <int N, class F> DI void sfor(F&& f) { sfor_(f, std::make_integer_sequence<int, N>{}); }
#define PIN() __builtin_amdgcn_sched_barrier(0)
DI Lane mklane() {
  Lane L; asm volatile("v_mbcnt_lo_u32_b32 %0, -1, 0\n\tv_mbcnt_hi_u32_b32 %0, -1, %0" : "=v"(L.l));
  L.i = L.l & 15u; L.g = L.l >> 4; L.qp = L.i >> 2; L.p = L.i & 3u; return L;
}

template <int NQ> DI void q_write(const f32x4 (&acc)[2][NQ], unsigned qbuf, const Lane& L, unsigned s, unsigned q0) {
#pragma unroll
  for (int u = 0; u < 2; ++u) {
    const unsigned a = qbuf + 256u * (16u * q0 + L.i) + 16u * ((2u * (2u * s + u) + (L.g >> 1)) ^ L.i) + 8u * (L.g & 1u);
#pragma unroll
    for (int qt = 0; qt < NQ; ++qt) {
      u32x2 w = {cvtpk(acc[u][qt][0] * QSCALE, acc[u][qt][1] * QSCALE), cvtpk(acc[u][qt][2] * QSCALE, acc[u][qt][3] * QSCALE)};
      *(AS3 u32x2*)(a + 4096u * qt) = w;
    }
  }
}

struct Src { srd_t x, w0, w1, k0, v0, k1, v1; };

template <bool LOADER> DI void phase0(const Src& S, unsigned wid, unsigned lds) {
  constexpr int NQ = LOADER ? 3 : 4;
  const Lane L = mklane();
  const unsigned vk = 256u * (L.l >> 4) + 16u * ((L.l & 15u) ^ (4u * (wid & 3u) + (L.l >> 4))), vv = 256u * (L.l >> 4) + 16u * ((L.l & 15u) ^ (2u * (4u * (wid & 1u) + (L.l >> 4))));
  const unsigned s = wid & 3u, q0 = LOADER ? 4u : 0u;
  const unsigned lr = L.l >> 4, lc = L.l & 15u;
  const unsigned vw = 8192u * lr + 16u * (lc ^ (2u * (lr | ((wid & 1u) << 2))));
  const unsigned vx0 = 2048u * (L.l >> 3) + 16u * ((L.l & 7u) ^ lr) + 32768u * wid, vx1 = (vx0 ^ 64u) + 16384u;
  const unsigned h = L.qp | ((L.g & 1u) << 2);
  const unsigned wa0 = REG + 256u * (8u * L.g + L.qp) + 16u * ((2u * (2u * s) + (L.p >> 1)) ^ (2u * h)) + 8u * (L.p & 1u), wa1 = wa0 ^ 32u;
  const unsigned xa0 = 128u * L.i + 16u * (L.g ^ (L.i >> 1)) + 2048u * q0, xa1 = xa0 ^ 64u;
  const unsigned rd[2][4] = {{lds + wa0, lds + wa1, lds + xa0, lds + xa1}, {lds + 65536u + wa0, lds + 65536u + wa1, lds + 65536u + xa0, lds + 65536u + xa1}};
  auto issue_g = [&](int i) {
    const unsigned st = lds + (unsigned)((i + 2) & 3) * 2u * REG;
    dma<0>(st + 2048u * wid, vx0, S.x, 128u * i);
    dma<0>(st + 2048u * wid + 1024u, vx1, S.x, 128u * i);
    dma<W_NT>(st + REG + 2048u * wid, vw, S.w0, 524288u * i + 65536u * wid);
    dma<W_NT>(st + REG + 2048u * wid + 1024u, vw, S.w0, 524288u * i + 65536u * wid + 32768u);
  };
  auto issue_kv = [&](int t, unsigned slot) {
    const unsigned w = wid & 3u;
#pragma unroll
    for (int q = 0; q < 4; ++q) dma<KV_NT>(slot + 1024u * w + 4096u * q, vk, S.k0, 16384u * t + 1024u * w + 4096u * q);
#pragma unroll
    for (int q = 0; q < 4; ++q) dma<KV_NT>(slot + REG + 1024u * w + 4096u * q, vv, S.v0, 16384u * t + 1024u * w + 4096u * q);
  };
  f32x4 acc[2][NQ];
#pragma unroll
  for (int u = 0; u < 2; ++u)
#pragma unroll
    for (int qt = 0; qt < NQ; ++qt) acc[u][qt] = f32x4{0.f, 0.f, 0.f, 0.f};
  bf16x8 A0[2], B0[NQ], A1[2][2], B1[2][NQ];
  issue_g(0); issue_g(1); issue_g(2);
  if constexpr (LOADER) issue_kv(0, lds + 8u * REG);
  sfor<16>([&](auto I) {
    constexpr int i = I.v, st = (i + 2) & 3, o = (st & 1) * 32768;
    if constexpr (i <= 2) { if constexpr (LOADER) wait_vm<16>(); else wait_vm<8>(); }
    else if constexpr (i <= 13) wait_vm<8>();
    else if constexpr (i == 14) { if constexpr (LOADER) wait_vm<12>(); else wait_vm<4>(); }
    else { if constexpr (LOADER) wait_vm<16>(); else wait_vm<0>(); }
    bar(); PIN();
    const unsigned* r = rd[st >> 1];
    A0[0] = cat(ldstr(r[0] + o), ldstr(r[0] + o + 1024)); A0[1] = cat(ldstr(r[1] + o), ldstr(r[1] + o + 1024));
#pragma unroll
    for (int qt = 0; qt < NQ; ++qt) B0[qt] = lds128(r[2] + o + 2048 * qt);
    PIN();
    A1[i & 1][0] = cat(ldstr(r[0] + o + 8192), ldstr(r[0] + o + 9216)); A1[i & 1][1] = cat(ldstr(r[1] + o + 8192), ldstr(r[1] + o + 9216));
#pragma unroll
    for (int qt = 0; qt < NQ; ++qt) B1[i & 1][qt] = lds128(r[3] + o + 2048 * qt);
    PIN();
    if constexpr (i + 3 <= 15) issue_g(i + 3);
    if constexpr (LOADER) { if constexpr (i == 13) issue_kv(1, lds + 4u * REG); if constexpr (i == 14) issue_kv(2, lds + 6u * REG); }
    PIN();
    if constexpr (i > 0) {
#pragma unroll
      for (int qt = 0; qt < NQ; ++qt) { acc[0][qt] = mfma(A1[(i - 1) & 1][0], B1[(i - 1) & 1][qt], acc[0][qt]); acc[1][qt] = mfma(A1[(i - 1) & 1][1], B1[(i - 1) & 1][qt], acc[1][qt]); }
      PIN();
    }
#pragma unroll
    for (int qt = 0; qt < NQ; ++qt) { acc[0][qt] = mfma(A0[0], B0[qt], acc[0][qt]); acc[1][qt] = mfma(A0[1], B0[qt], acc[1][qt]); }
    PIN();
  });
#pragma unroll
  for (int qt = 0; qt < NQ; ++qt) { acc[0][qt] = mfma(A1[1][0], B1[1][qt], acc[0][qt]); acc[1][qt] = mfma(A1[1][1], B1[1][qt], acc[1][qt]); }
  q_write<NQ>(acc, lds, L, s, q0);
}

template <int NQ> struct Att {
  unsigned ka[4], va[8];
  unsigned kslot, vslot, lo, hi;
  bf16x8 Q[NQ][4], F[8], P[2][NQ], ones;
  f32x4 O[8][NQ], Ls[NQ], negm[NQ];
};
template <int NQ, int N> DI void ld_k(Att<NQ>& a) { a.F[N & 7] = lds128(a.ka[N & 3] + 4096 * (N >> 2)); }
template <int NQ, int N> DI void ld_v(Att<NQ>& a) { a.F[N & 7] = cat(ldstr(a.va[N & 7] + 8192 * (N >> 3)), ldstr(a.va[N & 7] + 8192 * (N >> 3) + 4096)); }
template <int NQ> DI void adv_k(Att<NQ>& a) { const unsigned d = a.kslot == a.hi ? 0u - 4u * REG : 2u * REG; a.kslot += d;
#pragma unroll
  for (int s = 0; s < 4; ++s) a.ka[s] += d; }
template <int NQ> DI void adv_v(Att<NQ>& a) { const unsigned d = a.vslot == a.hi ? 0u - 4u * REG : 2u * REG; a.vslot += d;
#pragma unroll
  for (int s = 0; s < 8; ++s) a.va[s] += d; }
template <int NQ, int PR> DI void sm_pair(Att<NQ>& a, const f32x4 (&S)[4][NQ]) {
  constexpr int ks = PR / (4 * NQ), qt = (PR >> 2) % NQ, w = PR & 3, kt = 2 * ks + (w >> 1), r0 = 2 * (w & 1);
  u32x4 u = __builtin_bit_cast(u32x4, a.P[ks][qt]);
  u[w] = cvtpk(__builtin_amdgcn_exp2f(S[kt][qt][r0]), __builtin_amdgcn_exp2f(S[kt][qt][r0 + 1]));
  a.P[ks][qt] = __builtin_bit_cast(bf16x8, u);
}
template <int NQ, int MODE> DI void att_step(Att<NQ>& a, const f32x4 (&Sp)[4][NQ], f32x4 (&Sn)[4][NQ]) {
  if constexpr (MODE != 3) {
    sfor<16>([&](auto I) {
      constexpr int n = I.v, kt = n >> 2, s = n & 3;
#pragma unroll
      for (int qt = 0; qt < NQ; ++qt) Sn[kt][qt] = mfma(a.F[n & 7], a.Q[qt][s], s ? Sn[kt][qt] : a.negm[qt]);
      if constexpr (n + 6 < 16) ld_k<NQ, n + 6>(a); else if constexpr (MODE != 0) ld_v<NQ, n + 6 - 16>(a);
      if constexpr (n == 9) adv_k<NQ>(a);
      if constexpr (MODE != 0) {
        if constexpr (NQ == 2) { if constexpr ((n & 3) != 3) sm_pair<NQ, n - (n >> 2)>(a, Sp); }
        else if constexpr ((n & 1) == 0) sm_pair<NQ, (n >> 1)>(a, Sp);
      }
      PIN();
    });
  } else {
    sfor<(NQ == 2 ? 12 : 8)>([&](auto I) { sm_pair<NQ, I.v>(a, Sp); });
    PIN();
  }
  float m0[NQ], m1[NQ];
  auto maxstep = [&](auto J) {
    constexpr int j = J.v, qt = NQ == 2 ? j >> 2 : 0, c = NQ == 2 ? j & 3 : j >> 1;
    if constexpr (NQ == 2 || (j & 1) == 0) m0[qt] = c ? max3f(m0[qt], Sn[c][qt][0], Sn[c][qt][1]) : max2f(Sn[0][qt][0], Sn[0][qt][1]);
    if constexpr (NQ == 2 || (j & 1) == 1) m1[qt] = c ? max3f(m1[qt], Sn[c][qt][2], Sn[c][qt][3]) : max2f(Sn[0][qt][2], Sn[0][qt][3]);
  };
  if constexpr (MODE != 0) {
    sfor<16>([&](auto I) {
      constexpr int n = I.v, ks = n >> 3, dt = n & 7;
#pragma unroll
      for (int qt = 0; qt < NQ; ++qt) a.O[dt][qt] = mfma(a.F[n & 7], a.P[ks][qt], a.O[dt][qt]);
      if constexpr (n + 6 < 16) ld_v<NQ, n + 6>(a);
      if constexpr (n == 9) adv_v<NQ>(a);
      if constexpr (NQ == 2 && n < 4) sm_pair<NQ, 12 + n>(a, Sp);
      if constexpr (MODE != 3 && n >= 8) maxstep(IC<n - 8>{});
      if constexpr (dt == 7) {
#pragma unroll
        for (int qt = 0; qt < NQ; ++qt) a.Ls[qt] = mfma(a.ones, a.P[ks][qt], a.Ls[qt]);
      }
      if constexpr (MODE != 3) {
        if constexpr (n == 11) { PIN(); bar(); PIN();
          if constexpr (MODE == 1) { ld_k<NQ, 0>(a); ld_k<NQ, 1>(a); ld_k<NQ, 2>(a); ld_k<NQ, 3>(a); }
          else { ld_v<NQ, 0>(a); ld_v<NQ, 1>(a); ld_v<NQ, 2>(a); ld_v<NQ, 3>(a); } }
        if constexpr (n == 12 || n == 13) { if constexpr (MODE == 1) ld_k<NQ, n - 8>(a); else ld_v<NQ, n - 8>(a); }
      }
      PIN();
    });
  } else {
    bar(); PIN();
    sfor<6>([&](auto I) { ld_k<NQ, I.v>(a); });
    PIN();
    sfor<8>(maxstep);
  }
  if constexpr (MODE != 3) {
    float mx[NQ];
    bool grow = MODE == 0;
#pragma unroll
    for (int qt = 0; qt < NQ; ++qt) { mx[qt] = max2f(m0[qt], m1[qt]); grow |= mx[qt] > THR; }
    if (MODE == 0 || __builtin_expect(__any((int)grow), 0)) {
#pragma unroll
      for (int qt = 0; qt < NQ; ++qt) {
        float m = max2f(mx[qt], __shfl_xor(mx[qt], 16)); m = max2f(m, __shfl_xor(m, 32));
        const float dl = MODE == 0 ? m : max2f(m, 0.f);
#pragma unroll
        for (int kt = 0; kt < 4; ++kt) Sn[kt][qt] -= dl;
        a.negm[qt] -= dl;
        if constexpr (MODE != 0) {
          const float f = __builtin_amdgcn_exp2f(-dl);
          a.Ls[qt] *= f;
#pragma unroll
          for (int dt = 0; dt < 8; ++dt) a.O[dt][qt] *= f;
        }
      }
    }
    PIN();
  }
}

template <int NQ> DI void attn_role(unsigned lds, unsigned wid, char* of, char* ws) {
  const Lane L = mklane();
  Att<NQ> a;
#pragma unroll
  for (int s = 0; s < 4; ++s) a.ka[s] = lds + 256u * L.i + 16u * ((4u * s + L.g) ^ L.i) + 8u * REG;
  const unsigned hv = 4u * (L.g & 1u) + L.qp;
#pragma unroll
  for (int dt = 0; dt < 8; ++dt) a.va[dt] = lds + 9u * REG + 256u * (4u * L.g + L.qp) + 32u * (dt ^ hv) + 16u * (L.p >> 1) + 8u * (L.p & 1u);
  a.kslot = 8u * REG; a.vslot = 8u * REG; a.hi = 8u * REG;
  const u32x4 onesu = {0x3F803F80u, 0x3F803F80u, 0x3F803F80u, 0x3F803F80u};
  a.ones = __builtin_bit_cast(bf16x8, onesu);
  asm volatile("" : "+v"(a.ones));
#pragma unroll 1
  for (int r = 0; r < 2; ++r) {
#pragma unroll
    for (int qt = 0; qt < NQ; ++qt) {
      a.Ls[qt] = f32x4{0.f, 0.f, 0.f, 0.f}; a.negm[qt] = f32x4{0.f, 0.f, 0.f, 0.f};
#pragma unroll
      for (int dt = 0; dt < 8; ++dt) a.O[dt][qt] = f32x4{0.f, 0.f, 0.f, 0.f};
#pragma unroll
      for (int ks = 0; ks < 2; ++ks) a.P[ks][qt] = a.ones;
    }
    f32x4 SA[4][NQ], SB[4][NQ];
    bar(); PIN();
#pragma unroll
    for (int qt = 0; qt < NQ; ++qt)
#pragma unroll
      for (int s = 0; s < 4; ++s) a.Q[qt][s] = lds128(a.ka[s] - a.kslot + 8192u * wid + 2u * REG * r + 4096u * qt);
    sfor<6>([&](auto I) { ld_k<NQ, I.v>(a); });
    PIN();
    att_step<NQ, 0>(a, SB, SA);
#pragma unroll 1
    for (int t = 0; t < 7; ++t) { att_step<NQ, 1>(a, SA, SB); att_step<NQ, 1>(a, SB, SA); }
    att_step<NQ, 2>(a, SA, SB);
    att_step<NQ, 3>(a, SB, SA);
    char* odst = of + r * 16 * 28672 + mklane().l * 16;
#pragma unroll
    for (int qt = 0; qt < NQ; ++qt) {
      const float inv = 1.0f / a.Ls[qt][0];
#pragma unroll
      for (int s = 0; s < 4; ++s) {
        const f32x4 x = a.O[2 * s][qt] * inv, y = a.O[2 * s + 1][qt] * inv;
        const u32x4 w = {cvtpk(x[0], x[1]), cvtpk(x[2], x[3]), cvtpk(y[0], y[1]), cvtpk(y[2], y[3])};
        *(u32x4*)(odst + (s * 7 + 2 * wid + qt) * 1024) = w;
      }
    }
    if (r == 0) STAMP(0, 2);
  }
}

DI void loader_role(const Src& S, unsigned w, unsigned lds) {
  const Lane L = mklane();
  const unsigned vk = 256u * (L.l >> 4) + 16u * ((L.l & 15u) ^ (4u * w + (L.l >> 4))), vv = 256u * (L.l >> 4) + 16u * ((L.l & 15u) ^ (2u * (4u * (w & 1u) + (L.l >> 4))));
  const unsigned lr = L.l >> 4, lc = L.l & 15u;
  const unsigned vw0 = 8192u * lr + 16u * (lc ^ (2u * lr)), vw1 = vw0 ^ 128u;
  const unsigned vx0 = 2048u * (L.l >> 3) + 16u * ((L.l & 7u) ^ lr) + 65536u * w;
  const unsigned vx[4] = {vx0, (vx0 ^ 64u) + 16384u, vx0 + 32768u, (vx0 ^ 64u) + 49152u};
  const unsigned h = L.qp | ((L.g & 1u) << 2);
  const unsigned wa0 = lds + REG + 256u * (8u * L.g + L.qp) + 16u * ((2u * (2u * w) + (L.p >> 1)) ^ (2u * h)) + 8u * (L.p & 1u), wa1 = wa0 ^ 32u;
  const unsigned xa0 = lds + 128u * L.i + 16u * (L.g ^ (L.i >> 1)), xa1 = xa0 ^ 64u;
  auto issue_g = [&](unsigned i, unsigned st) {
#pragma unroll
    for (int q = 0; q < 4; ++q) dma<0>(st + 4096u * w + 1024u * q, vx[q], S.x, 128u * i);
#pragma unroll
    for (int q = 0; q < 4; ++q) dma<W_NT>(st + REG + 4096u * w + 1024u * q, (q & 2) ? vw1 : vw0, S.w1, 524288u * i + 131072u * w + 32768u * q);
  };
  unsigned kslot = lds + 8u * REG, vslot = lds + 9u * REG;
  auto issue_k = [&](srd_t k, unsigned t) {
#pragma unroll
    for (int q = 0; q < 4; ++q) dma<KV_NT>(kslot + 1024u * w + 4096u * q, vk, k, 16384u * t + 1024u * w + 4096u * q);
    kslot = kslot == lds + 8u * REG ? lds + 4u * REG : kslot + 2u * REG;
  };
  auto issue_v = [&](srd_t v, unsigned t) {
#pragma unroll
    for (int q = 0; q < 4; ++q) dma<KV_NT>(vslot + 1024u * w + 4096u * q, vv, v, 16384u * t + 1024u * w + 4096u * q);
    vslot = vslot == lds + 9u * REG ? lds + 5u * REG : vslot + 2u * REG;
  };
  f32x4 acc[2][7];
#pragma unroll
  for (int u = 0; u < 2; ++u)
#pragma unroll
    for (int qt = 0; qt < 7; ++qt) acc[u][qt] = f32x4{0.f, 0.f, 0.f, 0.f};
  bf16x8 A[2][2], Bf[14];
  auto g_reads = [&](auto O_) { constexpr int o = O_.v;
    A[0][0] = cat(ldstr(wa0 + o), ldstr(wa0 + o + 1024)); A[0][1] = cat(ldstr(wa1 + o), ldstr(wa1 + o + 1024));
    sfor<6>([&](auto I) { Bf[I.v] = lds128(xa0 + o + 2048 * I.v); });
    A[1][0] = cat(ldstr(wa0 + o + 8192), ldstr(wa0 + o + 9216)); A[1][1] = cat(ldstr(wa1 + o + 8192), ldstr(wa1 + o + 9216));
    PIN(); };
  auto g_mm = [&](auto O_) { constexpr int o = O_.v;
    sfor<14>([&](auto I) { constexpr int n = I.v, ks = n / 7, qt = n % 7;
      acc[0][qt] = mfma(A[ks][0], Bf[n], acc[0][qt]); acc[1][qt] = mfma(A[ks][1], Bf[n], acc[1][qt]);
      if constexpr (n + 6 < 14) Bf[n + 6] = lds128((n + 6 < 7 ? xa0 : xa1) + o + 2048 * ((n + 6) % 7));
      PIN(); }); };
  constexpr int S0 = 0, S1 = 2 * REG;
  bar();
  issue_g(0, lds + S1);
  wait_vm<0>();
  bar(); PIN();
  g_reads(IC<S1>{}); issue_g(1, lds + S0); issue_k(S.k0, 3); PIN(); g_mm(IC<S1>{});
  wait_vm<4>();
#pragma unroll 1
  for (unsigned t = 2; t < 16; t += 2) {
    const bool last = t == 14;
    bar(); PIN();
    g_reads(IC<S0>{}); issue_g(t, lds + S1); issue_k(last ? S.k1 : S.k0, (t + 2) & 15u); issue_v(S.v0, t + 1); PIN(); g_mm(IC<S0>{});
    wait_vm<8>();
    bar(); PIN();
    g_reads(IC<S1>{}); issue_g(t + 1, lds + S0); issue_k(last ? S.k1 : S.k0, (t + 3) & 15u); issue_v(last ? S.v1 : S.v0, (t + 2) & 15u); PIN(); g_mm(IC<S1>{});
    wait_vm<8>();
  }
  bar(); PIN();
  g_reads(IC<S0>{}); issue_k(S.k1, 2); issue_v(S.v1, 1); PIN(); g_mm(IC<S0>{});
  q_write<7>(acc, lds + S1, L, w, 0);
  bar();
  wait_vm<8>();
#pragma unroll 1
  for (unsigned t = 1; t < 14; ++t) {
    bar();
    issue_k(S.k1, t + 2); issue_v(S.v1, t + 1);
    wait_vm<8>();
  }
  bar();
  issue_v(S.v1, 15); wait_vm<4>();
  bar();
  wait_vm<0>();
  bar();
}

__global__ __launch_bounds__(512, 2) void qattn_kernel(const char* xA, const char* kA, const char* vA, const char* wqA,
                                                       const char* xB, const char* kB, const char* vB, const char* wqB, char* ws) {
  constexpr unsigned lds = 0;
  const unsigned wid = rfl(threadIdx.x >> 6);
  const unsigned xcd = blockIdx.x & 7u, sl = blockIdx.x >> 3, br = xcd >> 2, j = (xcd & 3u) * 4u + (sl & 3u), b = sl >> 2;
  const char* x = br ? xB : xA; const char* kc = br ? kB : kA; const char* vc = br ? vB : vA; const char* wq = br ? wqB : wqA;
  Src S;
  S.x = mksrd(x + (size_t)b * SQ * DM * 2, SQ * DM * 2);
  S.w0 = mksrd(wq + j * 256u, DM * NH * DH * 2 - j * 256u);
  S.w1 = mksrd(wq + (16u + j) * 256u, DM * NH * DH * 2 - (16u + j) * 256u);
  const size_t it0 = (size_t)(b * NH + j) * SK * DH * 2, it1 = it0 + (size_t)16 * SK * DH * 2;
  S.k0 = mksrd(kc + it0, SK * DH * 2); S.v0 = mksrd(vc + it0, SK * DH * 2);
  S.k1 = mksrd(kc + it1, SK * DH * 2); S.v1 = mksrd(vc + it1, SK * DH * 2);
  char* of = ws + (size_t)((br * 8u + b) * NH + j) * 28672u;
  STAMP(0, 0);
  if (wid >= 4) {
    phase0<true>(S, wid, lds);
    STAMP(0, 1);
    loader_role(S, wid & 3u, lds);
  } else {
    phase0<false>(S, wid, lds);
    STAMP(0, 1);
    if (wid < 3) attn_role<2>(lds, wid, of, ws); else attn_role<1>(lds, wid, of, ws);
  }
  STAMP(0, 3);
}

__global__ __launch_bounds__(512, 2) void oproj_kernel(const char* woA, const char* woB, char* ws, char* out) {
  constexpr unsigned lds = 0;
  const unsigned wid = rfl(threadIdx.x >> 6), l = threadIdx.x & 63u, i = l & 15u, g = l >> 4, qp = i >> 2, p = i & 3u;
  const unsigned xcd = blockIdx.x & 7u, sl = blockIdx.x >> 3, br = xcd >> 2, j = (xcd & 3u) * 4u + (sl & 3u), b = sl >> 2;
  STAMP(1, 0);
  const char* wo = br ? woB : woA;
  const srd_t sw = mksrd(wo + j * 128u, NH * DH * DM * 2 - j * 128u);
  const char* of = ws + (size_t)((br * 8u + b) * NH + 4u * wid) * 28672u + l * 16u;
  const unsigned my = lds + wid * 16384u;
  const unsigned vw = 2048u * (l >> 3) + 16u * ((l & 7u) ^ (2u * ((l >> 4) & 3u)));
  unsigned wa[4];
#pragma unroll
  for (int nt = 0; nt < 4; ++nt) wa[nt] = my + 128u * (4u * g + qp) + 32u * (nt ^ (((4u * g + qp) >> 1) & 3u)) + 16u * (p >> 1) + 8u * (p & 1u);
  f32x4 acc[4][7];
#pragma unroll
  for (int nt = 0; nt < 4; ++nt)
#pragma unroll
    for (int qt = 0; qt < 7; ++qt) acc[nt][qt] = f32x4{0.f, 0.f, 0.f, 0.f};
  auto issue_w = [&](int k) {
#pragma unroll
    for (int n = 0; n < 4; ++n) dma<W_NT>(my + (k & 3) * 4096u + 1024u * n, vw, sw, (wid * 512u + k * 32u + 8u * n) * 2048u);
  };
  bf16x8 Bf[3][7];
  auto issue_o = [&](int k) {
#pragma unroll
    for (int qt = 0; qt < 7; ++qt)
      asm volatile("global_load_dwordx4 %0, %1, off offset:%2" : "=v"(Bf[k % 3][qt]) : "v"(of + k * 7168 + (qt >> 2) * 4096), "i"((qt & 3) * 1024) : "memory");
  };
  issue_w(0); issue_o(0); issue_w(1); issue_o(1);
#pragma unroll
  for (int k = 0; k < 16; ++k) {
    if (k + 2 < 16) { issue_w(k + 2); issue_o(k + 2); }
    if (k + 2 < 16) asm volatile("s_waitcnt vmcnt(22)" : "+v"(Bf[k % 3][0]), "+v"(Bf[k % 3][1]), "+v"(Bf[k % 3][2]), "+v"(Bf[k % 3][3]), "+v"(Bf[k % 3][4]), "+v"(Bf[k % 3][5]), "+v"(Bf[k % 3][6])::"memory");
    else if (k + 1 < 16) asm volatile("s_waitcnt vmcnt(11)" : "+v"(Bf[k % 3][0]), "+v"(Bf[k % 3][1]), "+v"(Bf[k % 3][2]), "+v"(Bf[k % 3][3]), "+v"(Bf[k % 3][4]), "+v"(Bf[k % 3][5]), "+v"(Bf[k % 3][6])::"memory");
    else asm volatile("s_waitcnt vmcnt(0)" : "+v"(Bf[k % 3][0]), "+v"(Bf[k % 3][1]), "+v"(Bf[k % 3][2]), "+v"(Bf[k % 3][3]), "+v"(Bf[k % 3][4]), "+v"(Bf[k % 3][5]), "+v"(Bf[k % 3][6])::"memory");
#pragma unroll
    for (int nt = 0; nt < 4; ++nt) {
      const bf16x8 a = cat(ldstr(wa[nt] + (k & 3) * 4096u), ldstr(wa[nt] + (k & 3) * 4096u + 2048u));
#pragma unroll
      for (int qt = 0; qt < 7; ++qt) acc[nt][qt] = mfma(a, Bf[k % 3][qt], acc[nt][qt]);
    }
    asm volatile("s_waitcnt lgkmcnt(0)" ::: "memory");
  }
  STAMP(1, 1);
  bar();
#pragma unroll
  for (int hlf = 0; hlf < 2; ++hlf) {
#pragma unroll
    for (int e = 0; e < 14; ++e) *(AS3 f32x4*)(lds + (wid * 14u + e) * 1024u + l * 16u) = acc[2 * hlf + e / 7][e % 7];
    bar();
#pragma unroll
    for (int rep = 0; rep < 2; ++rep) {
      const unsigned e = wid + 8u * rep;
      if (e < 14u) {
        f32x4 sum = *(AS3 f32x4*)(lds + e * 1024u + l * 16u);
#pragma unroll
        for (int w = 1; w < 8; ++w) sum += *(AS3 f32x4*)(lds + (w * 14u + e) * 1024u + l * 16u);
        const unsigned nt = 2u * hlf + (e >= 7u), qt = e >= 7u ? e - 7u : e;
        const u32x2 o = {cvtpk(sum[0], sum[1]), cvtpk(sum[2], sum[3])};
        *(u32x2*)(out + (size_t)(b * SQ + 16u * qt + i) * 4096u + br * 2048u + j * 128u + nt * 32u + g * 8u) = o;
      }
    }
    bar();
  }
  STAMP(1, 2);
}

extern "C" void kernel_launch(void* const* d_in, const int* in_sizes, int n_in, void* d_out, int out_size, void* d_ws, size_t ws_size, hipStream_t stream) {
  (void)hipFuncSetAttribute((const void*)qattn_kernel, hipFuncAttributeMaxDynamicSharedMemorySize, 163840);
  (void)hipFuncSetAttribute((const void*)oproj_kernel, hipFuncAttributeMaxDynamicSharedMemorySize, 131072);
  qattn_kernel<<<256, 512, 163840, stream>>>((const char*)d_in[0], (const char*)d_in[1], (const char*)d_in[2], (const char*)d_in[3],
                                             (const char*)d_in[5], (const char*)d_in[6], (const char*)d_in[7], (const char*)d_in[8], (char*)d_ws);
  oproj_kernel<<<256, 512, 131072, stream>>>((const char*)d_in[4], (const char*)d_in[9], (char*)d_ws, (char*)d_out);
}
```

```cpp
#include <hip/hip_runtime.h>
#define KERNEL_ASM \
"s_and_b32 s60, s2, 7\n\t" \
"s_lshr_b32 s38, s60, 2\n\t" \
"s_mul_i32 s3, s38, 40\n\t" \
"s_load_dwordx8 s[40:47], s[0:1], s3\n\t" \
"s_add_u32 s58, s3, 32\n\t" \
"s_load_dwordx2 s[48:49], s[0:1], s58\n\t" \
"s_load_dwordx4 s[52:55], s[0:1], 0x50\n\t" \
"v_lshrrev_b32 v1, 6, v0\n\t" \
"s_nop 0\n\t" \
"v_readfirstlane_b32 s32, v1\n\t" \
"v_and_b32 v0, 63, v0\n\t" \
"s_and_b32 s36, s60, 3\n\t" \
"s_lshr_b32 s61, s2, 3\n\t" \
"s_and_b32 s37, s61, 3\n\t" \
"s_lshr_b32 s35, s61, 2\n\t" \
"s_mul_i32 s39, s36, 8\n\t" \
"s_add_u32 s39, s39, s37\n\t" \
"s_lshl_b32 s81, s60, 1\n\t" \
"s_lshr_b32 s62, s61, 4\n\t" \
"s_add_u32 s81, s81, s62\n\t" \
"s_lshl_b32 s33, s35, 1\n\t" \
"s_lshl_b32 s62, s37, 2\n\t" \
"s_add_u32 s33, s33, s62\n\t" \
"s_mul_i32 s62, s61, 11\n\t" \
"s_add_u32 s62, s62, s60\n\t" \
"s_mul_i32 s63, s62, 10923\n\t" \
"s_lshr_b32 s63, s63, 16\n\t" \
"s_mul_i32 s63, s63, 6\n\t" \
"s_sub_u32 s34, s62, s63\n\t" \
"s_waitcnt lgkmcnt(0)\n\t" \
"s_mul_i32 s60, s35, 229376\n\t" \
"s_add_u32 s4, s40, s60\n\t" \
"s_addc_u32 s5, s41, 0\n\t" \
"s_and_b32 s5, s5, 0xffff\n\t" \
"s_mov_b32 s6, 0x38000\n\t" \
"s_mov_b32 s7, 0x20000\n\t" \
"s_lshl_b32 s60, s39, 8\n\t" \
"s_sub_u32 s61, 0x800000, s60\n\t" \
"s_add_u32 s8, s46, s60\n\t" \
"s_addc_u32 s9, s47, 0\n\t" \
"s_and_b32 s9, s9, 0xffff\n\t" \
"s_mov_b32 s10, s61\n\t" \
"s_mov_b32 s11, 0x20000\n\t" \
"s_lshl_b32 s60, s35, 5\n\t" \
"s_add_u32 s60, s60, s39\n\t" \
"s_lshl_b32 s60, s60, 18\n\t" \
"s_add_u32 s12, s42, s60\n\t" \
"s_addc_u32 s13, s43, 0\n\t" \
"s_and_b32 s13, s13, 0xffff\n\t" \
"s_mov_b32 s14, 0x140000\n\t" \
"s_mov_b32 s15, 0x20000\n\t" \
"s_add_u32 s16, s44, s60\n\t" \
"s_addc_u32 s17, s45, 0\n\t" \
"s_and_b32 s17, s17, 0xffff\n\t" \
"s_mov_b32 s18, 0x140000\n\t" \
"s_mov_b32 s19, 0x20000\n\t" \
"s_lshl_b32 s60, s37, 9\n\t" \
"s_sub_u32 s61, 0x800000, s60\n\t" \
"s_add_u32 s20, s48, s60\n\t" \
"s_addc_u32 s21, s49, 0\n\t" \
"s_and_b32 s21, s21, 0xffff\n\t" \
"s_mov_b32 s22, s61\n\t" \
"s_mov_b32 s23, 0x20000\n\t" \
"s_mov_b32 s28, s52\n\t" \
"s_and_b32 s29, s53, 0xffff\n\t" \
"s_mov_b32 s30, 0x380000\n\t" \
"s_mov_b32 s31, 0x20000\n\t" \
"s_mov_b32 s24, s54\n\t" \
"s_and_b32 s25, s55, 0xffff\n\t" \
"s_mov_b32 s26, 0x2100000\n\t" \
"s_mov_b32 s27, 0x20000\n\t" \
"s_lshl_b32 s60, s38, 3\n\t" \
"s_add_u32 s60, s60, s35\n\t" \
"s_mul_i32 s82, s60, 917504\n\t" \
"s_mul_i32 s56, s39, 28672\n\t" \
"s_add_u32 s56, s56, s82\n\t" \
"s_lshl_b32 s60, s60, 12\n\t" \
"s_add_u32 s60, s60, 0x2000000\n\t" \
"s_lshl_b32 s61, s36, 10\n\t" \
"s_add_u32 s61, s61, s60\n\t" \
"s_lshl_b32 s62, s37, 8\n\t" \
"s_add_u32 s83, s61, s62\n\t" \
"s_add_u32 s62, s62, s60\n\t" \
"v_lshrrev_b32 v1, 2, v0\n\t" \
"v_lshlrev_b32 v1, 8, v1\n\t" \
"v_add_u32 v1, s61, v1\n\t" \
"v_lshlrev_b32 v2, 10, v0\n\t" \
"v_add_u32 v2, s62, v2\n\t" \
"v_add_u32 v2, 0xffffc000, v2\n\t" \
"v_cmp_gt_u32 vcc, 16, v0\n\t" \
"s_nop 1\n\t" \
"v_cndmask_b32 v1, v2, v1, vcc\n\t" \
"v_mov_b32 v2, 0x40000000\n\t" \
"v_cmp_gt_u32 vcc, 20, v0\n\t" \
"s_nop 1\n\t" \
"v_cndmask_b32 v254, v2, v1, vcc\n\t" \
"s_and_b32 s71, s32, 1\n\t" \
"s_mul_i32 s72, s71, 7168\n\t" \
"s_lshl_b32 s78, s71, 13\n\t" \
"s_add_u32 s73, s78, 0x4000\n\t" \
"v_and_b32 v1, 15, v0\n\t" \
"v_lshrrev_b32 v2, 4, v0\n\t" \
"v_bfe_u32 v3, v0, 2, 2\n\t" \
"v_and_b32 v4, 3, v0\n\t" \
"s_cmp_ge_u32 s32, 4\n\t" \
"s_cbranch_scc1 L_gemm_%=\n\t" \
"s_cmp_ge_u32 s32, 2\n\t" \
"s_cbranch_scc1 L_p0idle_%=\n\t" \
"v_lshrrev_b32 v5, 3, v0\n\t" \
"v_lshlrev_b32 v5, 11, v5\n\t" \
"v_and_b32 v6, 7, v0\n\t" \
"v_xor_b32 v6, v6, v2\n\t" \
"v_lshl_add_u32 v5, v6, 4, v5\n\t" \
"s_mul_i32 s60, s71, 114688\n\t" \
"v_add_u32 v5, s60, v5\n\t" \
"s_lshl_b32 s61, s71, 6\n\t" \
"v_xor_b32 v8, s61, v5\n\t" \
"v_xor_b32 v5, 64, v8\n\t" \
"v_add_u32 v9, 15360, v5\n\t" \
"v_add_u32 v10, 30720, v8\n\t" \
"v_add_u32 v11, 46080, v5\n\t" \
"v_lshlrev_b32 v5, 1, v2\n\t" \
"v_xor_b32 v5, v5, v1\n\t" \
"v_lshlrev_b32 v5, 4, v5\n\t" \
"v_lshl_add_u32 v5, v2, 13, v5\n\t" \
"s_lshl_b32 s60, s71, 18\n\t" \
"v_add_u32 v12, s60, v5\n\t" \
"v_xor_b32 v5, 0x80, v12\n\t" \
"v_add_u32 v13, 31744, v12\n\t" \
"v_add_u32 v14, 63488, v5\n\t" \
"v_add_u32 v15, 95232, v5\n\t" \
"s_mov_b32 s70, s33\n\t" \
"s_and_b32 s60, s70, 15\n\t" \
"s_lshl_b32 s61, s60, 7\n\t" \
"s_add_u32 m0, s72, 0x8000\n\t" \
"s_lshl_b32 s62, s60, 19\n\t" \
"buffer_load_dwordx4 v8, s[4:7], s61 offen offset:0 lds\n\t" \
"buffer_load_dwordx4 v9, s[4:7], s61 offen offset:1024 lds\n\t" \
"buffer_load_dwordx4 v10, s[4:7], s61 offen offset:2048 lds\n\t" \
"buffer_load_dwordx4 v11, s[4:7], s61 offen offset:3072 lds\n\t" \
"s_add_u32 m0, m0, 4096\n\t" \
"s_add_u32 s61, s61, 0x10000\n\t" \
"buffer_load_dwordx4 v8, s[4:7], s61 offen offset:0 lds\n\t" \
"buffer_load_dwordx4 v9, s[4:7], s61 offen offset:1024 lds\n\t" \
"buffer_load_dwordx4 v10, s[4:7], s61 offen offset:2048 lds\n\t" \
"s_add_u32 m0, s73, 0x8000\n\t" \
"s_nop 0\n\t" \
"buffer_load_dwordx4 v12, s[8:11], s62 offen offset:0 lds\n\t" \
"buffer_load_dwordx4 v13, s[8:11], s62 offen offset:1024 lds\n\t" \
"buffer_load_dwordx4 v14, s[8:11], s62 offen offset:2048 lds\n\t" \
"buffer_load_dwordx4 v15, s[8:11], s62 offen offset:3072 lds\n\t" \
"s_add_u32 m0, m0, 4096\n\t" \
"s_add_u32 s62, s62, 0x20000\n\t" \
"buffer_load_dwordx4 v12, s[8:11], s62 offen offset:0 lds\n\t" \
"buffer_load_dwordx4 v13, s[8:11], s62 offen offset:1024 lds\n\t" \
"buffer_load_dwordx4 v14, s[8:11], s62 offen offset:2048 lds\n\t" \
"buffer_load_dwordx4 v15, s[8:11], s62 offen offset:3072 lds\n\t" \
"s_add_u32 s70, s70, 1\n\t" \
"s_and_b32 s60, s70, 15\n\t" \
"s_lshl_b32 s61, s60, 7\n\t" \
"s_add_u32 m0, s72, 0x10000\n\t" \
"s_lshl_b32 s62, s60, 19\n\t" \
"buffer_load_dwordx4 v8, s[4:7], s61 offen offset:0 lds\n\t" \
"buffer_load_dwordx4 v9, s[4:7], s61 offen offset:1024 lds\n\t" \
"buffer_load_dwordx4 v10, s[4:7], s61 offen offset:2048 lds\n\t" \
"buffer_load_dwordx4 v11, s[4:7], s61 offen offset:3072 lds\n\t" \
"s_add_u32 m0, m0, 4096\n\t" \
"s_add_u32 s61, s61, 0x10000\n\t" \
"buffer_load_dwordx4 v8, s[4:7], s61 offen offset:0 lds\n\t" \
"buffer_load_dwordx4 v9, s[4:7], s61 offen offset:1024 lds\n\t" \
"buffer_load_dwordx4 v10, s[4:7], s61 offen offset:2048 lds\n\t" \
"s_add_u32 m0, s73, 0x10000\n\t" \
"s_nop 0\n\t" \
"buffer_load_dwordx4 v12, s[8:11], s62 offen offset:0 lds\n\t" \
"buffer_load_dwordx4 v13, s[8:11], s62 offen offset:1024 lds\n\t" \
"buffer_load_dwordx4 v14, s[8:11], s62 offen offset:2048 lds\n\t" \
"buffer_load_dwordx4 v15, s[8:11], s62 offen offset:3072 lds\n\t" \
"s_add_u32 m0, m0, 4096\n\t" \
"s_add_u32 s62, s62, 0x20000\n\t" \
"buffer_load_dwordx4 v12, s[8:11], s62 offen offset:0 lds\n\t" \
"buffer_load_dwordx4 v13, s[8:11], s62 offen offset:1024 lds\n\t" \
"buffer_load_dwordx4 v14, s[8:11], s62 offen offset:2048 lds\n\t" \
"buffer_load_dwordx4 v15, s[8:11], s62 offen offset:3072 lds\n\t" \
"buffer_load_dwordx2 v[252:253], v254, s[24:27], 0 offen sc1\n\t" \
"s_mov_b32 s79, 0x0\n\t" \
"s_mov_b32 s64, 14\n\t" \
"L_p0st_%=:\n\t" \
"s_waitcnt vmcnt(15)\n\t" \
"s_barrier\n\t" \
"s_add_u32 s70, s70, 1\n\t" \
"s_and_b32 s60, s70, 15\n\t" \
"s_lshl_b32 s61, s60, 7\n\t" \
"s_add_u32 m0, s72, s79\n\t" \
"s_lshl_b32 s62, s60, 19\n\t" \
"buffer_load_dwordx4 v8, s[4:7], s61 offen offset:0 lds\n\t" \
"buffer_load_dwordx4 v9, s[4:7], s61 offen offset:1024 lds\n\t" \
"buffer_load_dwordx4 v10, s[4:7], s61 offen offset:2048 lds\n\t" \
"buffer_load_dwordx4 v11, s[4:7], s61 offen offset:3072 lds\n\t" \
"s_add_u32 m0, m0, 4096\n\t" \
"s_add_u32 s61, s61, 0x10000\n\t" \
"buffer_load_dwordx4 v8, s[4:7], s61 offen offset:0 lds\n\t" \
"buffer_load_dwordx4 v9, s[4:7], s61 offen offset:1024 lds\n\t" \
"buffer_load_dwordx4 v10, s[4:7], s61 offen offset:2048 lds\n\t" \
"s_add_u32 m0, s73, s79\n\t" \
"s_nop 0\n\t" \
"buffer_load_dwordx4 v12, s[8:11], s62 offen offset:0 lds\n\t" \
"buffer_load_dwordx4 v13, s[8:11], s62 offen offset:1024 lds\n\t" \
"buffer_load_dwordx4 v14, s[8:11], s62 offen offset:2048 lds\n\t" \
"buffer_load_dwordx4 v15, s[8:11], s62 offen offset:3072 lds\n\t" \
"s_add_u32 m0, m0, 4096\n\t" \
"s_add_u32 s62, s62, 0x20000\n\t" \
"buffer_load_dwordx4 v12, s[8:11], s62 offen offset:0 lds\n\t" \
"buffer_load_dwordx4 v13, s[8:11], s62 offen offset:1024 lds\n\t" \
"buffer_load_dwordx4 v14, s[8:11], s62 offen offset:2048 lds\n\t" \
"buffer_load_dwordx4 v15, s[8:11], s62 offen offset:3072 lds\n\t" \
"s_add_u32 s79, s79, 0x8000\n\t" \
"s_cmp_eq_u32 s79, 0x18000\n\t" \
"s_cselect_b32 s79, 0, s79\n\t" \
"s_sub_u32 s64, s64, 1\n\t" \
"s_cmp_lg_u32 s64, 0\n\t" \
"s_cbranch_scc1 L_p0st_%=\n\t" \
"s_waitcnt vmcnt(15)\n\t" \
"s_barrier\n\t" \
"s_waitcnt vmcnt(0)\n\t" \
"s_barrier\n\t" \
"s_branch L_attn_%=\n\t" \
"L_p0idle_%=:\n\t" \
"s_cmp_eq_u32 s32, 2\n\t" \
"s_cbranch_scc0 L_nopf_%=\n\t" \
"s_getpc_b64 s[40:41]\n\t" \
"L_pc_%=:\n\t" \
"s_and_b32 s41, s41, 0xffff\n\t" \
"s_mov_b32 s42, L_end_%=-L_pc_%=\n\t" \
"s_mov_b32 s43, 0x20000\n\t" \
"s_lshr_b32 s60, s2, 3\n\t" \
"s_lshl_b32 s60, s60, 11\n\t" \
"v_lshlrev_b32 v5, 7, v1\n\t" \
"v_add_u32 v5, s60, v5\n\t" \
"buffer_load_dword v6, v5, s[40:43], 0 offen\n\t" \
"L_nopf_%=:\n\t" \
"s_barrier\n\t" \
"s_barrier\n\t" \
"s_barrier\n\t" \
"s_barrier\n\t" \
"buffer_load_dwordx2 v[252:253], v254, s[24:27], 0 offen sc1\n\t" \
"s_barrier\n\t" \
"s_barrier\n\t" \
"s_barrier\n\t" \
"s_barrier\n\t" \
"s_barrier\n\t" \
"s_barrier\n\t" \
"s_barrier\n\t" \
"s_barrier\n\t" \
"s_barrier\n\t" \
"s_barrier\n\t" \
"s_barrier\n\t" \
"s_waitcnt vmcnt(0)\n\t" \
"s_barrier\n\t" \
"L_attn_%=:\n\t" \
"v_add_co_u32 v252, vcc, 1, v252\n\t" \
"s_nop 1\n\t" \
"v_addc_co_u32 v253, vcc, 0, v253, vcc\n\t" \
"s_lshl_b32 s60, s37, 2\n\t" \
"v_readlane_b32 s50, v252, s60\n\t" \
"v_readlane_b32 s51, v253, s60\n\t" \
"v_lshlrev_b32 v250, 4, v0\n\t" \
"v_lshlrev_b32 v5, 8, v1\n\t" \
"v_add_u32 v5, 0x20000, v5\n\t" \
"v_add_u32 v6, 0, v2\n\t" \
"v_xor_b32 v6, v6, v1\n\t" \
"v_lshl_add_u32 v220, v6, 4, v5\n\t" \
"v_add_u32 v6, 4, v2\n\t" \
"v_xor_b32 v6, v6, v1\n\t" \
"v_lshl_add_u32 v221, v6, 4, v5\n\t" \
"v_add_u32 v6, 8, v2\n\t" \
"v_xor_b32 v6, v6, v1\n\t" \
"v_lshl_add_u32 v222, v6, 4, v5\n\t" \
"v_add_u32 v6, 12, v2\n\t" \
"v_xor_b32 v6, v6, v1\n\t" \
"v_lshl_add_u32 v223, v6, 4, v5\n\t" \
"v_lshl_add_u32 v5, v2, 2, v3\n\t" \
"v_and_b32 v6, 7, v5\n\t" \
"v_lshlrev_b32 v5, 8, v5\n\t" \
"v_lshl_add_u32 v5, v6, 5, v5\n\t" \
"v_lshl_add_u32 v5, v4, 3, v5\n\t" \
"v_add_u32 v224, 0x24000, v5\n\t" \
"v_xor_b32 v225, 32, v224\n\t" \
"v_xor_b32 v226, 64, v224\n\t" \
"v_xor_b32 v227, 96, v224\n\t" \
"v_xor_b32 v228, 128, v224\n\t" \
"v_xor_b32 v229, 160, v224\n\t" \
"v_xor_b32 v230, 192, v224\n\t" \
"v_xor_b32 v231, 224, v224\n\t" \
"v_mov_b32 v216, 0x3f803f80\n\t" \
"v_mov_b32 v217, 0x3f803f80\n\t" \
"v_mov_b32 v218, 0x3f803f80\n\t" \
"v_mov_b32 v219, 0x3f803f80\n\t" \
"s_mov_b32 s65, 0x20000\n\t" \
"s_mov_b32 s66, 0x20000\n\t" \
"s_mov_b32 s68, 0\n\t" \
"s_mov_b32 s69, 0x41000000\n\t" \
"s_setprio 1\n\t" \
"s_cmp_eq_u32 s32, 3\n\t" \
"s_cbranch_scc1 L_item1_%=\n\t" \
"L_item2_%=:\n\t" \
"v_mov_b32 v72, 0\n\t" \
"v_mov_b32 v73, 0\n\t" \
"v_mov_b32 v74, 0\n\t" \
"v_mov_b32 v75, 0\n\t" \
"v_mov_b32 v76, 0\n\t" \
"v_mov_b32 v77, 0\n\t" \
"v_mov_b32 v78, 0\n\t" \
"v_mov_b32 v79, 0\n\t" \
"s_lshl_b32 s60, s32, 13\n\t" \
"s_lshl_b32 s61, s68, 15\n\t" \
"s_add_u32 s60, s60, s61\n\t" \
"s_sub_u32 s60, s60, s65\n\t" \
"v_add_u32 v240, s60, v220\n\t" \
"v_add_u32 v241, s60, v221\n\t" \
"v_add_u32 v242, s60, v222\n\t" \
"v_add_u32 v243, s60, v223\n\t" \
"s_barrier\n\t" \
"ds_read_b128 v[80:83], v240 offset:0\n\t" \
"ds_read_b128 v[96:99], v240 offset:4096\n\t" \
"ds_read_b128 v[84:87], v241 offset:0\n\t" \
"ds_read_b128 v[100:103], v241 offset:4096\n\t" \
"ds_read_b128 v[88:91], v242 offset:0\n\t" \
"ds_read_b128 v[104:107], v242 offset:4096\n\t" \
"ds_read_b128 v[92:95], v243 offset:0\n\t" \
"ds_read_b128 v[108:111], v243 offset:4096\n\t" \
"ds_read_b128 v[112:115], v220 offset:8192\n\t" \
"ds_read_b128 v[116:119], v221 offset:8192\n\t" \
"ds_read_b128 v[120:123], v222 offset:8192\n\t" \
"ds_read_b128 v[124:127], v223 offset:8192\n\t" \
"ds_read_b128 v[128:131], v220 offset:12288\n\t" \
"ds_read_b128 v[132:135], v221 offset:12288\n\t" \
"ds_read_b128 v[136:139], v222 offset:12288\n\t" \
"ds_read_b128 v[140:143], v223 offset:12288\n\t" \
"v_mov_b32 v64, 0\n\t" \
"v_mov_b32 v65, 0\n\t" \
"v_mov_b32 v66, 0\n\t" \
"v_mov_b32 v67, 0\n\t" \
"v_mov_b32 v0, 0\n\t" \
"v_mov_b32 v1, 0\n\t" \
"v_mov_b32 v2, 0\n\t" \
"v_mov_b32 v3, 0\n\t" \
"v_mov_b32 v8, 0\n\t" \
"v_mov_b32 v9, 0\n\t" \
"v_mov_b32 v10, 0\n\t" \
"v_mov_b32 v11, 0\n\t" \
"v_mov_b32 v16, 0\n\t" \
"v_mov_b32 v17, 0\n\t" \
"v_mov_b32 v18, 0\n\t" \
"v_mov_b32 v19, 0\n\t" \
"v_mov_b32 v24, 0\n\t" \
"v_mov_b32 v25, 0\n\t" \
"v_mov_b32 v26, 0\n\t" \
"v_mov_b32 v27, 0\n\t" \
"v_mov_b32 v32, 0\n\t" \
"v_mov_b32 v33, 0\n\t" \
"v_mov_b32 v34, 0\n\t" \
"v_mov_b32 v35, 0\n\t" \
"v_mov_b32 v40, 0\n\t" \
"v_mov_b32 v41, 0\n\t" \
"v_mov_b32 v42, 0\n\t" \
"v_mov_b32 v43, 0\n\t" \
"v_mov_b32 v48, 0\n\t" \
"v_mov_b32 v49, 0\n\t" \
"v_mov_b32 v50, 0\n\t" \
"v_mov_b32 v51, 0\n\t" \
"v_mov_b32 v56, 0\n\t" \
"v_mov_b32 v57, 0\n\t" \
"v_mov_b32 v58, 0\n\t" \
"v_mov_b32 v59, 0\n\t" \
"v_mov_b32 v68, 0\n\t" \
"v_mov_b32 v69, 0\n\t" \
"v_mov_b32 v70, 0\n\t" \
"v_mov_b32 v71, 0\n\t" \
"v_mov_b32 v4, 0\n\t" \
"v_mov_b32 v5, 0\n\t" \
"v_mov_b32 v6, 0\n\t" \
"v_mov_b32 v7, 0\n\t" \
"v_mov_b32 v12, 0\n\t" \
"v_mov_b32 v13, 0\n\t" \
"v_mov_b32 v14, 0\n\t" \
"v_mov_b32 v15, 0\n\t" \
"v_mov_b32 v20, 0\n\t" \
"v_mov_b32 v21, 0\n\t" \
"v_mov_b32 v22, 0\n\t" \
"v_mov_b32 v23, 0\n\t" \
"v_mov_b32 v28, 0\n\t" \
"v_mov_b32 v29, 0\n\t" \
"v_mov_b32 v30, 0\n\t" \
"v_mov_b32 v31, 0\n\t" \
"v_mov_b32 v36, 0\n\t" \
"v_mov_b32 v37, 0\n\t" \
"v_mov_b32 v38, 0\n\t" \
"v_mov_b32 v39, 0\n\t" \
"v_mov_b32 v44, 0\n\t" \
"v_mov_b32 v45, 0\n\t" \
"v_mov_b32 v46, 0\n\t" \
"v_mov_b32 v47, 0\n\t" \
"v_mov_b32 v52, 0\n\t" \
"v_mov_b32 v53, 0\n\t" \
"v_mov_b32 v54, 0\n\t" \
"v_mov_b32 v55, 0\n\t" \
"v_mov_b32 v60, 0\n\t" \
"v_mov_b32 v61, 0\n\t" \
"v_mov_b32 v62, 0\n\t" \
"v_mov_b32 v63, 0\n\t" \
"s_waitcnt lgkmcnt(7)\n\t" \
"v_mfma_f32_16x16x32_bf16 v[176:179], v[112:115], v[80:83], v[72:75]\n\t" \
"v_mfma_f32_16x16x32_bf16 v[180:183], v[112:115], v[96:99], v[76:79]\n\t" \
"s_waitcnt lgkmcnt(6)\n\t" \
"v_mfma_f32_16x16x32_bf16 v[176:179], v[116:119], v[84:87], v[176:179]\n\t" \
"v_mfma_f32_16x16x32_bf16 v[180:183], v[116:119], v[100:103], v[180:183]\n\t" \
"s_waitcnt lgkmcnt(5)\n\t" \
"v_mfma_f32_16x16x32_bf16 v[176:179], v[120:123], v[88:91], v[176:179]\n\t" \
"v_mfma_f32_16x16x32_bf16 v[180:183], v[120:123], v[104:107], v[180:183]\n\t" \
"s_waitcnt lgkmcnt(4)\n\t" \
"v_mfma_f32_16x16x32_bf16 v[176:179], v[124:127], v[92:95], v[176:179]\n\t" \
"v_mfma_f32_16x16x32_bf16 v[180:183], v[124:127], v[108:111], v[180:183]\n\t" \
"s_waitcnt lgkmcnt(3)\n\t" \
"v_mfma_f32_16x16x32_bf16 v[184:187], v[128:131], v[80:83], v[72:75]\n\t" \
"v_mfma_f32_16x16x32_bf16 v[188:191], v[128:131], v[96:99], v[76:79]\n\t" \
"s_waitcnt lgkmcnt(2)\n\t" \
"v_mfma_f32_16x16x32_bf16 v[184:187], v[132:135], v[84:87], v[184:187]\n\t" \
"v_mfma_f32_16x16x32_bf16 v[188:191], v[132:135], v[100:103], v[188:191]\n\t" \
"s_waitcnt lgkmcnt(1)\n\t" \
"v_mfma_f32_16x16x32_bf16 v[184:187], v[136:139], v[88:91], v[184:187]\n\t" \
"v_mfma_f32_16x16x32_bf16 v[188:191], v[136:139], v[104:107], v[188:191]\n\t" \
"s_waitcnt lgkmcnt(0)\n\t" \
"v_mfma_f32_16x16x32_bf16 v[184:187], v[140:143], v[92:95], v[184:187]\n\t" \
"v_mfma_f32_16x16x32_bf16 v[188:191], v[140:143], v[108:111], v[188:191]\n\t" \
"s_cmp_eq_u32 s68, 0\n\t" \
"s_cbranch_scc1 L_no11_%=\n\t" \
"s_lshl_b32 s61, s32, 11\n\t" \
"v_add_u32 v232, s61, v250\n\t" \
"ds_write_b128 v232, v[192:195] offset:0\n\t" \
"ds_write_b128 v232, v[144:147] offset:1024\n\t" \
"ds_write_b128 v232, v[196:199] offset:8192\n\t" \
"ds_write_b128 v232, v[148:151] offset:9216\n\t" \
"ds_write_b128 v232, v[200:203] offset:16384\n\t" \
"ds_write_b128 v232, v[152:155] offset:17408\n\t" \
"ds_write_b128 v232, v[204:207] offset:24576\n\t" \
"ds_write_b128 v232, v[156:159] offset:25600\n\t" \
"s_waitcnt lgkmcnt(0)\n\t" \
"L_no11_%=:\n\t" \
"s_barrier\n\t" \
"s_cmp_eq_u32 s65, 0x10000\n\t" \
"s_cselect_b32 s67, s65, 0xffff8000\n\t" \
"s_add_u32 s65, s65, s67\n\t" \
"v_add_u32 v220, s67, v220\n\t" \
"v_add_u32 v221, s67, v221\n\t" \
"v_add_u32 v222, s67, v222\n\t" \
"v_add_u32 v223, s67, v223\n\t" \
"s_cmp_eq_u32 s66, 0x10000\n\t" \
"s_cselect_b32 s67, s66, 0xffff8000\n\t" \
"s_add_u32 s66, s66, s67\n\t" \
"v_add_u32 v224, s67, v224\n\t" \
"ds_read_b128 v[112:115], v220 offset:0\n\t" \
"ds_read_b128 v[116:119], v221 offset:0\n\t" \
"ds_read_b64_tr_b16 v[144:145], v224 offset:0\n\t" \
"ds_read_b64_tr_b16 v[146:147], v224 offset:4096\n\t" \
"v_add_u32 v225, s67, v225\n\t" \
"ds_read_b128 v[120:123], v222 offset:0\n\t" \
"ds_read_b64_tr_b16 v[148:149], v225 offset:0\n\t" \
"ds_read_b64_tr_b16 v[150:151], v225 offset:4096\n\t" \
"v_add_u32 v226, s67, v226\n\t" \
"ds_read_b128 v[124:127], v223 offset:0\n\t" \
"ds_read_b64_tr_b16 v[152:153], v226 offset:0\n\t" \
"ds_read_b64_tr_b16 v[154:155], v226 offset:4096\n\t" \
"v_add_u32 v227, s67, v227\n\t" \
"ds_read_b128 v[128:131], v220 offset:4096\n\t" \
"ds_read_b64_tr_b16 v[156:157], v227 offset:0\n\t" \
"ds_read_b64_tr_b16 v[158:159], v227 offset:4096\n\t" \
"v_add_u32 v228, s67, v228\n\t" \
"ds_read_b128 v[132:135], v221 offset:4096\n\t" \
"ds_read_b64_tr_b16 v[160:161], v228 offset:0\n\t" \
"ds_read_b64_tr_b16 v[162:163], v228 offset:4096\n\t" \
"v_add_u32 v229, s67, v229\n\t" \
"ds_read_b128 v[136:139], v222 offset:4096\n\t" \
"ds_read_b64_tr_b16 v[164:165], v229 offset:0\n\t" \
"ds_read_b64_tr_b16 v[166:167], v229 offset:4096\n\t" \
"v_add_u32 v230, s67, v230\n\t" \
"ds_read_b128 v[140:143], v223 offset:4096\n\t" \
"ds_read_b64_tr_b16 v[168:169], v230 offset:0\n\t" \
"ds_read_b64_tr_b16 v[170:171], v230 offset:4096\n\t" \
"v_add_u32 v231, s67, v231\n\t" \
"ds_read_b64_tr_b16 v[172:173], v231 offset:0\n\t" \
"ds_read_b64_tr_b16 v[174:175], v231 offset:4096\n\t" \
"v_maximum3_f32 v236, v176, v177, v178\n\t" \
"v_maximum3_f32 v236, v236, v179, v184\n\t" \
"v_maximum3_f32 v236, v236, v185, v186\n\t" \
"v_maximum3_f32 v236, v236, v187, v187\n\t" \
"v_mov_b32 v237, v236\n\t" \
"s_nop 1\n\t" \
"v_permlane16_swap_b32 v236, v237\n\t" \
"v_maximum3_f32 v236, v236, v237, v237\n\t" \
"v_mov_b32 v237, v236\n\t" \
"s_nop 1\n\t" \
"v_permlane32_swap_b32 v236, v237\n\t" \
"v_maximum3_f32 v236, v236, v237, v237\n\t" \
"v_sub_f32 v176, v176, v236\n\t" \
"v_sub_f32 v177, v177, v236\n\t" \
"v_sub_f32 v178, v178, v236\n\t" \
"v_sub_f32 v179, v179, v236\n\t" \
"v_sub_f32 v184, v184, v236\n\t" \
"v_sub_f32 v185, v185, v236\n\t" \
"v_sub_f32 v186, v186, v236\n\t" \
"v_sub_f32 v187, v187, v236\n\t" \
"v_sub_f32 v72, v72, v236\n\t" \
"v_sub_f32 v73, v73, v236\n\t" \
"v_sub_f32 v74, v74, v236\n\t" \
"v_sub_f32 v75, v75, v236\n\t" \
"v_maximum3_f32 v236, v180, v181, v182\n\t" \
"v_maximum3_f32 v236, v236, v183, v188\n\t" \
"v_maximum3_f32 v236, v236, v189, v190\n\t" \
"v_maximum3_f32 v236, v236, v191, v191\n\t" \
"v_mov_b32 v237, v236\n\t" \
"s_nop 1\n\t" \
"v_permlane16_swap_b32 v236, v237\n\t" \
"v_maximum3_f32 v236, v236, v237, v237\n\t" \
"v_mov_b32 v237, v236\n\t" \
"s_nop 1\n\t" \
"v_permlane32_swap_b32 v236, v237\n\t" \
"v_maximum3_f32 v236, v236, v237, v237\n\t" \
"v_sub_f32 v180, v180, v236\n\t" \
"v_sub_f32 v181, v181, v236\n\t" \
"v_sub_f32 v182, v182, v236\n\t" \
"v_sub_f32 v183, v183, v236\n\t" \
"v_sub_f32 v188, v188, v236\n\t" \
"v_sub_f32 v189, v189, v236\n\t" \
"v_sub_f32 v190, v190, v236\n\t" \
"v_sub_f32 v191, v191, v236\n\t" \
"v_sub_f32 v76, v76, v236\n\t" \
"v_sub_f32 v77, v77, v236\n\t" \
"v_sub_f32 v78, v78, v236\n\t" \
"v_sub_f32 v79, v79, v236\n\t" \
"s_mov_b32 s64, 15\n\t" \
"L_loop2_%=:\n\t" \
"s_waitcnt lgkmcnt(14)\n\t" \
"v_exp_f32 v232, v176\n\t" \
"v_mfma_f32_16x16x32_bf16 v[192:195], v[112:115], v[80:83], v[72:75]\n\t" \
"v_exp_f32 v233, v177\n\t" \
"v_mfma_f32_16x16x32_bf16 v[196:199], v[112:115], v[96:99], v[76:79]\n\t" \
"v_exp_f32 v234, v178\n\t" \
"v_cvt_pk_bf16_f32 v208, v232, v233\n\t" \
"v_mfma_f32_16x16x32_bf16 v[192:195], v[116:119], v[84:87], v[192:195]\n\t" \
"v_exp_f32 v235, v179\n\t" \
"v_mfma_f32_16x16x32_bf16 v[196:199], v[116:119], v[100:103], v[196:199]\n\t" \
"v_exp_f32 v232, v184\n\t" \
"v_cvt_pk_bf16_f32 v209, v234, v235\n\t" \
"v_mfma_f32_16x16x32_bf16 v[192:195], v[120:123], v[88:91], v[192:195]\n\t" \
"v_exp_f32 v233, v185\n\t" \
"v_mfma_f32_16x16x32_bf16 v[196:199], v[120:123], v[104:107], v[196:199]\n\t" \
"s_waitcnt lgkmcnt(0)\n\t" \
"v_exp_f32 v234, v186\n\t" \
"v_cvt_pk_bf16_f32 v210, v232, v233\n\t" \
"v_mfma_f32_16x16x32_bf16 v[192:195], v[124:127], v[92:95], v[192:195]\n\t" \
"v_exp_f32 v235, v187\n\t" \
"v_mfma_f32_16x16x32_bf16 v[196:199], v[124:127], v[108:111], v[196:199]\n\t" \
"v_exp_f32 v232, v180\n\t" \
"v_cvt_pk_bf16_f32 v211, v234, v235\n\t" \
"v_mfma_f32_16x16x32_bf16 v[200:203], v[128:131], v[80:83], v[72:75]\n\t" \
"v_exp_f32 v233, v181\n\t" \
"v_mfma_f32_16x16x32_bf16 v[204:207], v[128:131], v[96:99], v[76:79]\n\t" \
"v_exp_f32 v234, v182\n\t" \
"v_cvt_pk_bf16_f32 v212, v232, v233\n\t" \
"v_mfma_f32_16x16x32_bf16 v[200:203], v[132:135], v[84:87], v[200:203]\n\t" \
"v_exp_f32 v235, v183\n\t" \
"v_mfma_f32_16x16x32_bf16 v[204:207], v[132:135], v[100:103], v[204:207]\n\t" \
"v_exp_f32 v232, v188\n\t" \
"v_cvt_pk_bf16_f32 v213, v234, v235\n\t" \
"v_mfma_f32_16x16x32_bf16 v[200:203], v[136:139], v[88:91], v[200:203]\n\t" \
"v_exp_f32 v233, v189\n\t" \
"v_mfma_f32_16x16x32_bf16 v[204:207], v[136:139], v[104:107], v[204:207]\n\t" \
"v_exp_f32 v234, v190\n\t" \
"v_cvt_pk_bf16_f32 v214, v232, v233\n\t" \
"v_mfma_f32_16x16x32_bf16 v[200:203], v[140:143], v[92:95], v[200:203]\n\t" \
"v_exp_f32 v235, v191\n\t" \
"v_mfma_f32_16x16x32_bf16 v[204:207], v[140:143], v[108:111], v[204:207]\n\t" \
"v_cvt_pk_bf16_f32 v215, v234, v235\n\t" \
"s_cmp_eq_u32 s65, 0x10000\n\t" \
"s_cselect_b32 s67, s65, 0xffff8000\n\t" \
"s_add_u32 s65, s65, s67\n\t" \
"v_mfma_f32_16x16x32_bf16 v[0:3], v[144:147], v[208:211], v[0:3]\n\t" \
"ds_read_b128 v[112:115], v220 offset:8192\n\t" \
"v_mfma_f32_16x16x32_bf16 v[4:7], v[144:147], v[212:215], v[4:7]\n\t" \
"v_mfma_f32_16x16x32_bf16 v[8:11], v[148:151], v[208:211], v[8:11]\n\t" \
"ds_read_b128 v[116:119], v221 offset:8192\n\t" \
"ds_read_b64_tr_b16 v[144:145], v224 offset:8192\n\t" \
"v_mfma_f32_16x16x32_bf16 v[12:15], v[148:151], v[212:215], v[12:15]\n\t" \
"ds_read_b64_tr_b16 v[146:147], v224 offset:12288\n\t" \
"v_maximum3_f32 v236, v192, v193, v194\n\t" \
"v_mfma_f32_16x16x32_bf16 v[16:19], v[152:155], v[208:211], v[16:19]\n\t" \
"ds_read_b128 v[120:123], v222 offset:8192\n\t" \
"ds_read_b64_tr_b16 v[148:149], v225 offset:8192\n\t" \
"v_mfma_f32_16x16x32_bf16 v[20:23], v[152:155], v[212:215], v[20:23]\n\t" \
"ds_read_b64_tr_b16 v[150:151], v225 offset:12288\n\t" \
"v_maximum3_f32 v236, v236, v195, v200\n\t" \
"v_mfma_f32_16x16x32_bf16 v[24:27], v[156:159], v[208:211], v[24:27]\n\t" \
"ds_read_b128 v[124:127], v223 offset:8192\n\t" \
"ds_read_b64_tr_b16 v[152:153], v226 offset:8192\n\t" \
"v_mfma_f32_16x16x32_bf16 v[28:31], v[156:159], v[212:215], v[28:31]\n\t" \
"ds_read_b64_tr_b16 v[154:155], v226 offset:12288\n\t" \
"v_maximum3_f32 v236, v236, v201, v202\n\t" \
"v_mfma_f32_16x16x32_bf16 v[32:35], v[160:163], v[208:211], v[32:35]\n\t" \
"ds_read_b128 v[128:131], v220 offset:12288\n\t" \
"ds_read_b64_tr_b16 v[156:157], v227 offset:8192\n\t" \
"v_mfma_f32_16x16x32_bf16 v[36:39], v[160:163], v[212:215], v[36:39]\n\t" \
"ds_read_b64_tr_b16 v[158:159], v227 offset:12288\n\t" \
"v_maximum3_f32 v236, v236, v203, v196\n\t" \
"v_mfma_f32_16x16x32_bf16 v[40:43], v[164:167], v[208:211], v[40:43]\n\t" \
"ds_read_b128 v[132:135], v221 offset:12288\n\t" \
"ds_read_b64_tr_b16 v[160:161], v228 offset:8192\n\t" \
"v_mfma_f32_16x16x32_bf16 v[44:47], v[164:167], v[212:215], v[44:47]\n\t" \
"ds_read_b64_tr_b16 v[162:163], v228 offset:12288\n\t" \
"v_maximum3_f32 v236, v236, v197, v198\n\t" \
"v_add_u32 v220, s67, v220\n\t" \
"v_mfma_f32_16x16x32_bf16 v[48:51], v[168:171], v[208:211], v[48:51]\n\t" \
"ds_read_b128 v[136:139], v222 offset:12288\n\t" \
"ds_read_b64_tr_b16 v[164:165], v229 offset:8192\n\t" \
"v_mfma_f32_16x16x32_bf16 v[52:55], v[168:171], v[212:215], v[52:55]\n\t" \
"ds_read_b64_tr_b16 v[166:167], v229 offset:12288\n\t" \
"v_maximum3_f32 v236, v236, v199, v204\n\t" \
"v_add_u32 v221, s67, v221\n\t" \
"v_mfma_f32_16x16x32_bf16 v[56:59], v[172:175], v[208:211], v[56:59]\n\t" \
"ds_read_b128 v[140:143], v223 offset:12288\n\t" \
"ds_read_b64_tr_b16 v[168:169], v230 offset:8192\n\t" \
"v_mfma_f32_16x16x32_bf16 v[60:63], v[172:175], v[212:215], v[60:63]\n\t" \
"ds_read_b64_tr_b16 v[170:171], v230 offset:12288\n\t" \
"v_maximum3_f32 v236, v236, v205, v206\n\t" \
"v_add_u32 v222, s67, v222\n\t" \
"v_mfma_f32_16x16x32_bf16 v[64:67], v[216:219], v[208:211], v[64:67]\n\t" \
"ds_read_b64_tr_b16 v[172:173], v231 offset:8192\n\t" \
"v_mfma_f32_16x16x32_bf16 v[68:71], v[216:219], v[212:215], v[68:71]\n\t" \
"ds_read_b64_tr_b16 v[174:175], v231 offset:12288\n\t" \
"v_maximum3_f32 v236, v236, v207, v207\n\t" \
"v_add_u32 v223, s67, v223\n\t" \
"v_cmp_lt_f32 vcc, s69, v236\n\t" \
"s_cbranch_vccnz L_rare2_%=\n\t" \
"L_back2_%=:\n\t" \
"s_waitcnt lgkmcnt(14)\n\t" \
"v_exp_f32 v232, v192\n\t" \
"v_mfma_f32_16x16x32_bf16 v[176:179], v[112:115], v[80:83], v[72:75]\n\t" \
"v_exp_f32 v233, v193\n\t" \
"v_mfma_f32_16x16x32_bf16 v[180:183], v[112:115], v[96:99], v[76:79]\n\t" \
"v_exp_f32 v234, v194\n\t" \
"v_cvt_pk_bf16_f32 v208, v232, v233\n\t" \
"v_mfma_f32_16x16x32_bf16 v[176:179], v[116:119], v[84:87], v[176:179]\n\t" \
"v_exp_f32 v235, v195\n\t" \
"v_mfma_f32_16x16x32_bf16 v[180:183], v[116:119], v[100:103], v[180:183]\n\t" \
"v_exp_f32 v232, v200\n\t" \
"v_cvt_pk_bf16_f32 v209, v234, v235\n\t" \
"v_mfma_f32_16x16x32_bf16 v[176:179], v[120:123], v[88:91], v[176:179]\n\t" \
"v_exp_f32 v233, v201\n\t" \
"v_mfma_f32_16x16x32_bf16 v[180:183], v[120:123], v[104:107], v[180:183]\n\t" \
"s_waitcnt lgkmcnt(0)\n\t" \
"s_barrier\n\t" \
"v_exp_f32 v234, v202\n\t" \
"v_cvt_pk_bf16_f32 v210, v232, v233\n\t" \
"v_mfma_f32_16x16x32_bf16 v[176:179], v[124:127], v[92:95], v[176:179]\n\t" \
"v_exp_f32 v235, v203\n\t" \
"v_mfma_f32_16x16x32_bf16 v[180:183], v[124:127], v[108:111], v[180:183]\n\t" \
"v_exp_f32 v232, v196\n\t" \
"v_cvt_pk_bf16_f32 v211, v234, v235\n\t" \
"v_mfma_f32_16x16x32_bf16 v[184:187], v[128:131], v[80:83], v[72:75]\n\t" \
"v_exp_f32 v233, v197\n\t" \
"v_mfma_f32_16x16x32_bf16 v[188:191], v[128:131], v[96:99], v[76:79]\n\t" \
"v_exp_f32 v234, v198\n\t" \
"v_cvt_pk_bf16_f32 v212, v232, v233\n\t" \
"v_mfma_f32_16x16x32_bf16 v[184:187], v[132:135], v[84:87], v[184:187]\n\t" \
"v_exp_f32 v235, v199\n\t" \
"v_mfma_f32_16x16x32_bf16 v[188:191], v[132:135], v[100:103], v[188:191]\n\t" \
"v_exp_f32 v232, v204\n\t" \
"v_cvt_pk_bf16_f32 v213, v234, v235\n\t" \
"v_mfma_f32_16x16x32_bf16 v[184:187], v[136:139], v[88:91], v[184:187]\n\t" \
"v_exp_f32 v233, v205\n\t" \
"v_mfma_f32_16x16x32_bf16 v[188:191], v[136:139], v[104:107], v[188:191]\n\t" \
"v_exp_f32 v234, v206\n\t" \
"v_cvt_pk_bf16_f32 v214, v232, v233\n\t" \
"v_mfma_f32_16x16x32_bf16 v[184:187], v[140:143], v[92:95], v[184:187]\n\t" \
"v_exp_f32 v235, v207\n\t" \
"v_mfma_f32_16x16x32_bf16 v[188:191], v[140:143], v[108:111], v[188:191]\n\t" \
"v_cvt_pk_bf16_f32 v215, v234, v235\n\t" \
"s_cmp_eq_u32 s66, 0x10000\n\t" \
"s_cselect_b32 s67, s66, 0xffff8000\n\t" \
"s_add_u32 s66, s66, s67\n\t" \
"v_add_u32 v224, s67, v224\n\t" \
"v_mfma_f32_16x16x32_bf16 v[0:3], v[144:147], v[208:211], v[0:3]\n\t" \
"ds_read_b128 v[112:115], v220 offset:0\n\t" \
"v_mfma_f32_16x16x32_bf16 v[4:7], v[144:147], v[212:215], v[4:7]\n\t" \
"v_mfma_f32_16x16x32_bf16 v[8:11], v[148:151], v[208:211], v[8:11]\n\t" \
"ds_read_b128 v[116:119], v221 offset:0\n\t" \
"ds_read_b64_tr_b16 v[144:145], v224 offset:0\n\t" \
"v_mfma_f32_16x16x32_bf16 v[12:15], v[148:151], v[212:215], v[12:15]\n\t" \
"ds_read_b64_tr_b16 v[146:147], v224 offset:4096\n\t" \
"v_maximum3_f32 v236, v176, v177, v178\n\t" \
"v_add_u32 v225, s67, v225\n\t" \
"v_mfma_f32_16x16x32_bf16 v[16:19], v[152:155], v[208:211], v[16:19]\n\t" \
"ds_read_b128 v[120:123], v222 offset:0\n\t" \
"ds_read_b64_tr_b16 v[148:149], v225 offset:0\n\t" \
"v_mfma_f32_16x16x32_bf16 v[20:23], v[152:155], v[212:215], v[20:23]\n\t" \
"ds_read_b64_tr_b16 v[150:151], v225 offset:4096\n\t" \
"v_maximum3_f32 v236, v236, v179, v184\n\t" \
"v_add_u32 v226, s67, v226\n\t" \
"v_mfma_f32_16x16x32_bf16 v[24:27], v[156:159], v[208:211], v[24:27]\n\t" \
"ds_read_b128 v[124:127], v223 offset:0\n\t" \
"ds_read_b64_tr_b16 v[152:153], v226 offset:0\n\t" \
"v_mfma_f32_16x16x32_bf16 v[28:31], v[156:159], v[212:215], v[28:31]\n\t" \
"ds_read_b64_tr_b16 v[154:155], v226 offset:4096\n\t" \
"v_maximum3_f32 v236, v236, v185, v186\n\t" \
"v_add_u32 v227, s67, v227\n\t" \
"v_mfma_f32_16x16x32_bf16 v[32:35], v[160:163], v[208:211], v[32:35]\n\t" \
"ds_read_b128 v[128:131], v220 offset:4096\n\t" \
"ds_read_b64_tr_b16 v[156:157], v227 offset:0\n\t" \
"v_mfma_f32_16x16x32_bf16 v[36:39], v[160:163], v[212:215], v[36:39]\n\t" \
"ds_read_b64_tr_b16 v[158:159], v227 offset:4096\n\t" \
"v_maximum3_f32 v236, v236, v187, v180\n\t" \
"v_add_u32 v228, s67, v228\n\t" \
"v_mfma_f32_16x16x32_bf16 v[40:43], v[164:167], v[208:211], v[40:43]\n\t" \
"ds_read_b128 v[132:135], v221 offset:4096\n\t" \
"ds_read_b64_tr_b16 v[160:161], v228 offset:0\n\t" \
"v_mfma_f32_16x16x32_bf16 v[44:47], v[164:167], v[212:215], v[44:47]\n\t" \
"ds_read_b64_tr_b16 v[162:163], v228 offset:4096\n\t" \
"v_maximum3_f32 v236, v236, v181, v182\n\t" \
"v_add_u32 v229, s67, v229\n\t" \
"v_mfma_f32_16x16x32_bf16 v[48:51], v[168:171], v[208:211], v[48:51]\n\t" \
"ds_read_b128 v[136:139], v222 offset:4096\n\t" \
"ds_read_b64_tr_b16 v[164:165], v229 offset:0\n\t" \
"v_mfma_f32_16x16x32_bf16 v[52:55], v[168:171], v[212:215], v[52:55]\n\t" \
"ds_read_b64_tr_b16 v[166:167], v229 offset:4096\n\t" \
"v_maximum3_f32 v236, v236, v183, v188\n\t" \
"v_add_u32 v230, s67, v230\n\t" \
"v_mfma_f32_16x16x32_bf16 v[56:59], v[172:175], v[208:211], v[56:59]\n\t" \
"ds_read_b128 v[140:143], v223 offset:4096\n\t" \
"ds_read_b64_tr_b16 v[168:169], v230 offset:0\n\t" \
"v_mfma_f32_16x16x32_bf16 v[60:63], v[172:175], v[212:215], v[60:63]\n\t" \
"ds_read_b64_tr_b16 v[170:171], v230 offset:4096\n\t" \
"v_maximum3_f32 v236, v236, v189, v190\n\t" \
"v_add_u32 v231, s67, v231\n\t" \
"v_mfma_f32_16x16x32_bf16 v[64:67], v[216:219], v[208:211], v[64:67]\n\t" \
"ds_read_b64_tr_b16 v[172:173], v231 offset:0\n\t" \
"v_mfma_f32_16x16x32_bf16 v[68:71], v[216:219], v[212:215], v[68:71]\n\t" \
"ds_read_b64_tr_b16 v[174:175], v231 offset:4096\n\t" \
"v_maximum3_f32 v236, v236, v191, v191\n\t" \
"v_cmp_lt_f32 vcc, s69, v236\n\t" \
"s_cbranch_vccnz L_rare3_%=\n\t" \
"L_back3_%=:\n\t" \
"s_sub_u32 s64, s64, 1\n\t" \
"s_cmp_lg_u32 s64, 0\n\t" \
"s_cbranch_scc1 L_loop2_%=\n\t" \
"s_waitcnt lgkmcnt(14)\n\t" \
"v_exp_f32 v232, v176\n\t" \
"v_mfma_f32_16x16x32_bf16 v[192:195], v[112:115], v[80:83], v[72:75]\n\t" \
"v_exp_f32 v233, v177\n\t" \
"v_mfma_f32_16x16x32_bf16 v[196:199], v[112:115], v[96:99], v[76:79]\n\t" \
"v_exp_f32 v234, v178\n\t" \
"v_cvt_pk_bf16_f32 v208, v232, v233\n\t" \
"v_mfma_f32_16x16x32_bf16 v[192:195], v[116:119], v[84:87], v[192:195]\n\t" \
"v_exp_f32 v235, v179\n\t" \
"v_mfma_f32_16x16x32_bf16 v[196:199], v[116:119], v[100:103], v[196:199]\n\t" \
"v_exp_f32 v232, v184\n\t" \
"v_cvt_pk_bf16_f32 v209, v234, v235\n\t" \
"v_mfma_f32_16x16x32_bf16 v[192:195], v[120:123], v[88:91], v[192:195]\n\t" \
"v_exp_f32 v233, v185\n\t" \
"v_mfma_f32_16x16x32_bf16 v[196:199], v[120:123], v[104:107], v[196:199]\n\t" \
"s_waitcnt lgkmcnt(0)\n\t" \
"v_exp_f32 v234, v186\n\t" \
"v_cvt_pk_bf16_f32 v210, v232, v233\n\t" \
"v_mfma_f32_16x16x32_bf16 v[192:195], v[124:127], v[92:95], v[192:195]\n\t" \
"v_exp_f32 v235, v187\n\t" \
"v_mfma_f32_16x16x32_bf16 v[196:199], v[124:127], v[108:111], v[196:199]\n\t" \
"v_exp_f32 v232, v180\n\t" \
"v_cvt_pk_bf16_f32 v211, v234, v235\n\t" \
"v_mfma_f32_16x16x32_bf16 v[200:203], v[128:131], v[80:83], v[72:75]\n\t" \
"v_exp_f32 v233, v181\n\t" \
"v_mfma_f32_16x16x32_bf16 v[204:207], v[128:131], v[96:99], v[76:79]\n\t" \
"v_exp_f32 v234, v182\n\t" \
"v_cvt_pk_bf16_f32 v212, v232, v233\n\t" \
"v_mfma_f32_16x16x32_bf16 v[200:203], v[132:135], v[84:87], v[200:203]\n\t" \
"v_exp_f32 v235, v183\n\t" \
"v_mfma_f32_16x16x32_bf16 v[204:207], v[132:135], v[100:103], v[204:207]\n\t" \
"v_exp_f32 v232, v188\n\t" \
"v_cvt_pk_bf16_f32 v213, v234, v235\n\t" \
"v_mfma_f32_16x16x32_bf16 v[200:203], v[136:139], v[88:91], v[200:203]\n\t" \
"v_exp_f32 v233, v189\n\t" \
"v_mfma_f32_16x16x32_bf16 v[204:207], v[136:139], v[104:107], v[204:207]\n\t" \
"v_exp_f32 v234, v190\n\t" \
"v_cvt_pk_bf16_f32 v214, v232, v233\n\t" \
"v_mfma_f32_16x16x32_bf16 v[200:203], v[140:143], v[92:95], v[200:203]\n\t" \
"v_exp_f32 v235, v191\n\t" \
"v_mfma_f32_16x16x32_bf16 v[204:207], v[140:143], v[108:111], v[204:207]\n\t" \
"v_cvt_pk_bf16_f32 v215, v234, v235\n\t" \
"v_mfma_f32_16x16x32_bf16 v[0:3], v[144:147], v[208:211], v[0:3]\n\t" \
"s_nop 0\n\t" \
"v_mfma_f32_16x16x32_bf16 v[4:7], v[144:147], v[212:215], v[4:7]\n\t" \
"v_mfma_f32_16x16x32_bf16 v[8:11], v[148:151], v[208:211], v[8:11]\n\t" \
"ds_read_b64_tr_b16 v[144:145], v224 offset:8192\n\t" \
"v_mfma_f32_16x16x32_bf16 v[12:15], v[148:151], v[212:215], v[12:15]\n\t" \
"ds_read_b64_tr_b16 v[146:147], v224 offset:12288\n\t" \
"v_maximum3_f32 v236, v192, v193, v194\n\t" \
"v_mfma_f32_16x16x32_bf16 v[16:19], v[152:155], v[208:211], v[16:19]\n\t" \
"ds_read_b64_tr_b16 v[148:149], v225 offset:8192\n\t" \
"v_mfma_f32_16x16x32_bf16 v[20:23], v[152:155], v[212:215], v[20:23]\n\t" \
"ds_read_b64_tr_b16 v[150:151], v225 offset:12288\n\t" \
"v_maximum3_f32 v236, v236, v195, v200\n\t" \
"v_mfma_f32_16x16x32_bf16 v[24:27], v[156:159], v[208:211], v[24:27]\n\t" \
"ds_read_b64_tr_b16 v[152:153], v226 offset:8192\n\t" \
"v_mfma_f32_16x16x32_bf16 v[28:31], v[156:159], v[212:215], v[28:31]\n\t" \
"ds_read_b64_tr_b16 v[154:155], v226 offset:12288\n\t" \
"v_maximum3_f32 v236, v236, v201, v202\n\t" \
"v_mfma_f32_16x16x32_bf16 v[32:35], v[160:163], v[208:211], v[32:35]\n\t" \
"ds_read_b64_tr_b16 v[156:157], v227 offset:8192\n\t" \
"v_mfma_f32_16x16x32_bf16 v[36:39], v[160:163], v[212:215], v[36:39]\n\t" \
"ds_read_b64_tr_b16 v[158:159], v227 offset:12288\n\t" \
"v_maximum3_f32 v236, v236, v203, v196\n\t" \
"v_mfma_f32_16x16x32_bf16 v[40:43], v[164:167], v[208:211], v[40:43]\n\t" \
"ds_read_b64_tr_b16 v[160:161], v228 offset:8192\n\t" \
"v_mfma_f32_16x16x32_bf16 v[44:47], v[164:167], v[212:215], v[44:47]\n\t" \
"ds_read_b64_tr_b16 v[162:163], v228 offset:12288\n\t" \
"v_maximum3_f32 v236, v236, v197, v198\n\t" \
"v_mfma_f32_16x16x32_bf16 v[48:51], v[168:171], v[208:211], v[48:51]\n\t" \
"ds_read_b64_tr_b16 v[164:165], v229 offset:8192\n\t" \
"v_mfma_f32_16x16x32_bf16 v[52:55], v[168:171], v[212:215], v[52:55]\n\t" \
"ds_read_b64_tr_b16 v[166:167], v229 offset:12288\n\t" \
"v_maximum3_f32 v236, v236, v199, v204\n\t" \
"v_mfma_f32_16x16x32_bf16 v[56:59], v[172:175], v[208:211], v[56:59]\n\t" \
"ds_read_b64_tr_b16 v[168:169], v230 offset:8192\n\t" \
"v_mfma_f32_16x16x32_bf16 v[60:63], v[172:175], v[212:215], v[60:63]\n\t" \
"ds_read_b64_tr_b16 v[170:171], v230 offset:12288\n\t" \
"v_maximum3_f32 v236, v236, v205, v206\n\t" \
"v_mfma_f32_16x16x32_bf16 v[64:67], v[216:219], v[208:211], v[64:67]\n\t" \
"ds_read_b64_tr_b16 v[172:173], v231 offset:8192\n\t" \
"v_mfma_f32_16x16x32_bf16 v[68:71], v[216:219], v[212:215], v[68:71]\n\t" \
"ds_read_b64_tr_b16 v[174:175], v231 offset:12288\n\t" \
"v_maximum3_f32 v236, v236, v207, v207\n\t" \
"v_cmp_lt_f32 vcc, s69, v236\n\t" \
"s_cbranch_vccnz L_rare4_%=\n\t" \
"L_back4_%=:\n\t" \
"s_waitcnt lgkmcnt(0)\n\t" \
"s_barrier\n\t" \
"s_cmp_eq_u32 s68, 1\n\t" \
"s_cbranch_scc1 L_last2_%=\n\t" \
"v_exp_f32 v232, v192\n\t" \
"v_exp_f32 v233, v193\n\t" \
"v_exp_f32 v234, v194\n\t" \
"v_cvt_pk_bf16_f32 v208, v232, v233\n\t" \
"v_exp_f32 v235, v195\n\t" \
"v_exp_f32 v232, v200\n\t" \
"v_cvt_pk_bf16_f32 v209, v234, v235\n\t" \
"v_exp_f32 v233, v201\n\t" \
"v_exp_f32 v234, v202\n\t" \
"v_cvt_pk_bf16_f32 v210, v232, v233\n\t" \
"v_exp_f32 v235, v203\n\t" \
"v_exp_f32 v232, v196\n\t" \
"v_cvt_pk_bf16_f32 v211, v234, v235\n\t" \
"s_nop 1\n\t" \
"v_mfma_f32_16x16x32_bf16 v[64:67], v[216:219], v[208:211], v[64:67]\n\t" \
"v_mfma_f32_16x16x32_bf16 v[0:3], v[144:147], v[208:211], v[0:3]\n\t" \
"v_exp_f32 v233, v197\n\t" \
"v_mfma_f32_16x16x32_bf16 v[8:11], v[148:151], v[208:211], v[8:11]\n\t" \
"v_exp_f32 v234, v198\n\t" \
"v_cvt_pk_bf16_f32 v212, v232, v233\n\t" \
"v_mfma_f32_16x16x32_bf16 v[16:19], v[152:155], v[208:211], v[16:19]\n\t" \
"v_exp_f32 v235, v199\n\t" \
"v_mfma_f32_16x16x32_bf16 v[24:27], v[156:159], v[208:211], v[24:27]\n\t" \
"v_exp_f32 v232, v204\n\t" \
"v_cvt_pk_bf16_f32 v213, v234, v235\n\t" \
"v_mfma_f32_16x16x32_bf16 v[32:35], v[160:163], v[208:211], v[32:35]\n\t" \
"v_exp_f32 v233, v205\n\t" \
"v_mfma_f32_16x16x32_bf16 v[40:43], v[164:167], v[208:211], v[40:43]\n\t" \
"v_exp_f32 v234, v206\n\t" \
"v_cvt_pk_bf16_f32 v214, v232, v233\n\t" \
"v_mfma_f32_16x16x32_bf16 v[48:51], v[168:171], v[208:211], v[48:51]\n\t" \
"v_exp_f32 v235, v207\n\t" \
"v_mfma_f32_16x16x32_bf16 v[56:59], v[172:175], v[208:211], v[56:59]\n\t" \
"v_cvt_pk_bf16_f32 v215, v234, v235\n\t" \
"v_rcp_f32 v236, v64\n\t" \
"s_nop 0\n\t" \
"v_mfma_f32_16x16x32_bf16 v[68:71], v[216:219], v[212:215], v[68:71]\n\t" \
"v_mfma_f32_16x16x32_bf16 v[4:7], v[144:147], v[212:215], v[4:7]\n\t" \
"v_pk_mul_f32 v[0:1], v[0:1], v[236:237] op_sel_hi:[1,0]\n\t" \
"v_pk_mul_f32 v[2:3], v[2:3], v[236:237] op_sel_hi:[1,0]\n\t" \
"v_cvt_pk_bf16_f32 v192, v0, v1\n\t" \
"v_cvt_pk_bf16_f32 v193, v2, v3\n\t" \
"v_mfma_f32_16x16x32_bf16 v[12:15], v[148:151], v[212:215], v[12:15]\n\t" \
"v_pk_mul_f32 v[8:9], v[8:9], v[236:237] op_sel_hi:[1,0]\n\t" \
"v_pk_mul_f32 v[10:11], v[10:11], v[236:237] op_sel_hi:[1,0]\n\t" \
"v_cvt_pk_bf16_f32 v194, v8, v9\n\t" \
"v_cvt_pk_bf16_f32 v195, v10, v11\n\t" \
"v_mfma_f32_16x16x32_bf16 v[20:23], v[152:155], v[212:215], v[20:23]\n\t" \
"v_pk_mul_f32 v[16:17], v[16:17], v[236:237] op_sel_hi:[1,0]\n\t" \
"v_pk_mul_f32 v[18:19], v[18:19], v[236:237] op_sel_hi:[1,0]\n\t" \
"v_cvt_pk_bf16_f32 v196, v16, v17\n\t" \
"v_cvt_pk_bf16_f32 v197, v18, v19\n\t" \
"v_mfma_f32_16x16x32_bf16 v[28:31], v[156:159], v[212:215], v[28:31]\n\t" \
"v_pk_mul_f32 v[24:25], v[24:25], v[236:237] op_sel_hi:[1,0]\n\t" \
"v_pk_mul_f32 v[26:27], v[26:27], v[236:237] op_sel_hi:[1,0]\n\t" \
"v_cvt_pk_bf16_f32 v198, v24, v25\n\t" \
"v_cvt_pk_bf16_f32 v199, v26, v27\n\t" \
"v_mfma_f32_16x16x32_bf16 v[36:39], v[160:163], v[212:215], v[36:39]\n\t" \
"v_pk_mul_f32 v[32:33], v[32:33], v[236:237] op_sel_hi:[1,0]\n\t" \
"v_pk_mul_f32 v[34:35], v[34:35], v[236:237] op_sel_hi:[1,0]\n\t" \
"v_cvt_pk_bf16_f32 v200, v32, v33\n\t" \
"v_cvt_pk_bf16_f32 v201, v34, v35\n\t" \
"v_mfma_f32_16x16x32_bf16 v[44:47], v[164:167], v[212:215], v[44:47]\n\t" \
"v_pk_mul_f32 v[40:41], v[40:41], v[236:237] op_sel_hi:[1,0]\n\t" \
"v_pk_mul_f32 v[42:43], v[42:43], v[236:237] op_sel_hi:[1,0]\n\t" \
"v_cvt_pk_bf16_f32 v202, v40, v41\n\t" \
"v_cvt_pk_bf16_f32 v203, v42, v43\n\t" \
"v_rcp_f32 v238, v68\n\t" \
"v_mfma_f32_16x16x32_bf16 v[52:55], v[168:171], v[212:215], v[52:55]\n\t" \
"v_pk_mul_f32 v[48:49], v[48:49], v[236:237] op_sel_hi:[1,0]\n\t" \
"v_pk_mul_f32 v[50:51], v[50:51], v[236:237] op_sel_hi:[1,0]\n\t" \
"v_cvt_pk_bf16_f32 v204, v48, v49\n\t" \
"v_cvt_pk_bf16_f32 v205, v50, v51\n\t" \
"v_mfma_f32_16x16x32_bf16 v[60:63], v[172:175], v[212:215], v[60:63]\n\t" \
"v_pk_mul_f32 v[56:57], v[56:57], v[236:237] op_sel_hi:[1,0]\n\t" \
"v_pk_mul_f32 v[58:59], v[58:59], v[236:237] op_sel_hi:[1,0]\n\t" \
"v_cvt_pk_bf16_f32 v206, v56, v57\n\t" \
"v_cvt_pk_bf16_f32 v207, v58, v59\n\t" \
"v_pk_mul_f32 v[4:5], v[4:5], v[238:239] op_sel_hi:[1,0]\n\t" \
"v_pk_mul_f32 v[6:7], v[6:7], v[238:239] op_sel_hi:[1,0]\n\t" \
"v_cvt_pk_bf16_f32 v144, v4, v5\n\t" \
"v_cvt_pk_bf16_f32 v145, v6, v7\n\t" \
"v_pk_mul_f32 v[12:13], v[12:13], v[238:239] op_sel_hi:[1,0]\n\t" \
"v_pk_mul_f32 v[14:15], v[14:15], v[238:239] op_sel_hi:[1,0]\n\t" \
"v_cvt_pk_bf16_f32 v146, v12, v13\n\t" \
"v_cvt_pk_bf16_f32 v147, v14, v15\n\t" \
"v_pk_mul_f32 v[20:21], v[20:21], v[238:239] op_sel_hi:[1,0]\n\t" \
"v_pk_mul_f32 v[22:23], v[22:23], v[238:239] op_sel_hi:[1,0]\n\t" \
"v_cvt_pk_bf16_f32 v148, v20, v21\n\t" \
"v_cvt_pk_bf16_f32 v149, v22, v23\n\t" \
"v_pk_mul_f32 v[28:29], v[28:29], v[238:239] op_sel_hi:[1,0]\n\t" \
"v_pk_mul_f32 v[30:31], v[30:31], v[238:239] op_sel_hi:[1,0]\n\t" \
"v_cvt_pk_bf16_f32 v150, v28, v29\n\t" \
"v_cvt_pk_bf16_f32 v151, v30, v31\n\t" \
"v_pk_mul_f32 v[36:37], v[36:37], v[238:239] op_sel_hi:[1,0]\n\t" \
"v_pk_mul_f32 v[38:39], v[38:39], v[238:239] op_sel_hi:[1,0]\n\t" \
"v_cvt_pk_bf16_f32 v152, v36, v37\n\t" \
"v_cvt_pk_bf16_f32 v153, v38, v39\n\t" \
"v_pk_mul_f32 v[44:45], v[44:45], v[238:239] op_sel_hi:[1,0]\n\t" \
"v_pk_mul_f32 v[46:47], v[46:47], v[238:239] op_sel_hi:[1,0]\n\t" \
"v_cvt_pk_bf16_f32 v154, v44, v45\n\t" \
"v_cvt_pk_bf16_f32 v155, v46, v47\n\t" \
"v_pk_mul_f32 v[52:53], v[52:53], v[238:239] op_sel_hi:[1,0]\n\t" \
"v_pk_mul_f32 v[54:55], v[54:55], v[238:239] op_sel_hi:[1,0]\n\t" \
"v_cvt_pk_bf16_f32 v156, v52, v53\n\t" \
"v_cvt_pk_bf16_f32 v157, v54, v55\n\t" \
"v_pk_mul_f32 v[60:61], v[60:61], v[238:239] op_sel_hi:[1,0]\n\t" \
"v_pk_mul_f32 v[62:63], v[62:63], v[238:239] op_sel_hi:[1,0]\n\t" \
"v_cvt_pk_bf16_f32 v158, v60, v61\n\t" \
"v_cvt_pk_bf16_f32 v159, v62, v63\n\t" \
"s_mov_b32 s68, 1\n\t" \
"s_branch L_item2_%=\n\t" \
"L_last2_%=:\n\t" \
"v_exp_f32 v232, v192\n\t" \
"v_exp_f32 v233, v193\n\t" \
"v_exp_f32 v234, v194\n\t" \
"v_cvt_pk_bf16_f32 v208, v232, v233\n\t" \
"v_exp_f32 v235, v195\n\t" \
"v_exp_f32 v232, v200\n\t" \
"v_cvt_pk_bf16_f32 v209, v234, v235\n\t" \
"v_exp_f32 v233, v201\n\t" \
"v_exp_f32 v234, v202\n\t" \
"v_cvt_pk_bf16_f32 v210, v232, v233\n\t" \
"v_exp_f32 v235, v203\n\t" \
"v_exp_f32 v232, v196\n\t" \
"v_cvt_pk_bf16_f32 v211, v234, v235\n\t" \
"s_lshl_b32 s61, s32, 11\n\t" \
"v_add_u32 v240, s61, v250\n\t" \
"v_add_u32 v241, 0x10000, v240\n\t" \
"v_add_u32 v242, 0x20000, v240\n\t" \
"s_lshl_b32 s61, s32, 2\n\t" \
"s_add_u32 s61, s61, 0x7c10\n\t" \
"v_mov_b32 v243, s61\n\t" \
"v_mfma_f32_16x16x32_bf16 v[64:67], v[216:219], v[208:211], v[64:67]\n\t" \
"v_mfma_f32_16x16x32_bf16 v[0:3], v[144:147], v[208:211], v[0:3]\n\t" \
"v_exp_f32 v233, v197\n\t" \
"v_mfma_f32_16x16x32_bf16 v[8:11], v[148:151], v[208:211], v[8:11]\n\t" \
"v_exp_f32 v234, v198\n\t" \
"v_cvt_pk_bf16_f32 v212, v232, v233\n\t" \
"v_mfma_f32_16x16x32_bf16 v[16:19], v[152:155], v[208:211], v[16:19]\n\t" \
"v_exp_f32 v235, v199\n\t" \
"v_mfma_f32_16x16x32_bf16 v[24:27], v[156:159], v[208:211], v[24:27]\n\t" \
"v_exp_f32 v232, v204\n\t" \
"v_cvt_pk_bf16_f32 v213, v234, v235\n\t" \
"v_mfma_f32_16x16x32_bf16 v[32:35], v[160:163], v[208:211], v[32:35]\n\t" \
"v_exp_f32 v233, v205\n\t" \
"v_mfma_f32_16x16x32_bf16 v[40:43], v[164:167], v[208:211], v[40:43]\n\t" \
"v_exp_f32 v234, v206\n\t" \
"v_cvt_pk_bf16_f32 v214, v232, v233\n\t" \
"v_mfma_f32_16x16x32_bf16 v[48:51], v[168:171], v[208:211], v[48:51]\n\t" \
"v_exp_f32 v235, v207\n\t" \
"v_mfma_f32_16x16x32_bf16 v[56:59], v[172:175], v[208:211], v[56:59]\n\t" \
"v_cvt_pk_bf16_f32 v215, v234, v235\n\t" \
"v_rcp_f32 v236, v64\n\t" \
"s_nop 0\n\t" \
"v_mfma_f32_16x16x32_bf16 v[68:71], v[216:219], v[212:215], v[68:71]\n\t" \
"v_mfma_f32_16x16x32_bf16 v[4:7], v[144:147], v[212:215], v[4:7]\n\t" \
"v_pk_mul_f32 v[0:1], v[0:1], v[236:237] op_sel_hi:[1,0]\n\t" \
"v_pk_mul_f32 v[2:3], v[2:3], v[236:237] op_sel_hi:[1,0]\n\t" \
"v_cvt_pk_bf16_f32 v192, v0, v1\n\t" \
"v_cvt_pk_bf16_f32 v193, v2, v3\n\t" \
"v_mfma_f32_16x16x32_bf16 v[12:15], v[148:151], v[212:215], v[12:15]\n\t" \
"v_pk_mul_f32 v[8:9], v[8:9], v[236:237] op_sel_hi:[1,0]\n\t" \
"v_pk_mul_f32 v[10:11], v[10:11], v[236:237] op_sel_hi:[1,0]\n\t" \
"v_cvt_pk_bf16_f32 v194, v8, v9\n\t" \
"v_cvt_pk_bf16_f32 v195, v10, v11\n\t" \
"v_mfma_f32_16x16x32_bf16 v[20:23], v[152:155], v[212:215], v[20:23]\n\t" \
"ds_write_b128 v242, v[192:195] offset:0\n\t" \
"v_pk_mul_f32 v[16:17], v[16:17], v[236:237] op_sel_hi:[1,0]\n\t" \
"v_pk_mul_f32 v[18:19], v[18:19], v[236:237] op_sel_hi:[1,0]\n\t" \
"v_cvt_pk_bf16_f32 v196, v16, v17\n\t" \
"v_cvt_pk_bf16_f32 v197, v18, v19\n\t" \
"v_mfma_f32_16x16x32_bf16 v[28:31], v[156:159], v[212:215], v[28:31]\n\t" \
"v_pk_mul_f32 v[24:25], v[24:25], v[236:237] op_sel_hi:[1,0]\n\t" \
"v_pk_mul_f32 v[26:27], v[26:27], v[236:237] op_sel_hi:[1,0]\n\t" \
"v_cvt_pk_bf16_f32 v198, v24, v25\n\t" \
"v_cvt_pk_bf16_f32 v199, v26, v27\n\t" \
"v_mfma_f32_16x16x32_bf16 v[36:39], v[160:163], v[212:215], v[36:39]\n\t" \
"ds_write_b128 v242, v[196:199] offset:8192\n\t" \
"v_pk_mul_f32 v[32:33], v[32:33], v[236:237] op_sel_hi:[1,0]\n\t" \
"v_pk_mul_f32 v[34:35], v[34:35], v[236:237] op_sel_hi:[1,0]\n\t" \
"v_cvt_pk_bf16_f32 v200, v32, v33\n\t" \
"v_cvt_pk_bf16_f32 v201, v34, v35\n\t" \
"v_mfma_f32_16x16x32_bf16 v[44:47], v[164:167], v[212:215], v[44:47]\n\t" \
"v_pk_mul_f32 v[40:41], v[40:41], v[236:237] op_sel_hi:[1,0]\n\t" \
"v_pk_mul_f32 v[42:43], v[42:43], v[236:237] op_sel_hi:[1,0]\n\t" \
"v_cvt_pk_bf16_f32 v202, v40, v41\n\t" \
"v_cvt_pk_bf16_f32 v203, v42, v43\n\t" \
"v_rcp_f32 v238, v68\n\t" \
"v_mfma_f32_16x16x32_bf16 v[52:55], v[168:171], v[212:215], v[52:55]\n\t" \
"ds_write_b128 v241, v[200:203] offset:32768\n\t" \
"v_pk_mul_f32 v[48:49], v[48:49], v[236:237] op_sel_hi:[1,0]\n\t" \
"v_pk_mul_f32 v[50:51], v[50:51], v[236:237] op_sel_hi:[1,0]\n\t" \
"v_cvt_pk_bf16_f32 v204, v48, v49\n\t" \
"v_cvt_pk_bf16_f32 v205, v50, v51\n\t" \
"v_mfma_f32_16x16x32_bf16 v[60:63], v[172:175], v[212:215], v[60:63]\n\t" \
"v_pk_mul_f32 v[56:57], v[56:57], v[236:237] op_sel_hi:[1,0]\n\t" \
"v_pk_mul_f32 v[58:59], v[58:59], v[236:237] op_sel_hi:[1,0]\n\t" \
"v_cvt_pk_bf16_f32 v206, v56, v57\n\t" \
"v_cvt_pk_bf16_f32 v207, v58, v59\n\t" \
"ds_write_b128 v241, v[204:207] offset:40960\n\t" \
"v_pk_mul_f32 v[4:5], v[4:5], v[238:239] op_sel_hi:[1,0]\n\t" \
"v_pk_mul_f32 v[6:7], v[6:7], v[238:239] op_sel_hi:[1,0]\n\t" \
"v_cvt_pk_bf16_f32 v144, v4, v5\n\t" \
"v_cvt_pk_bf16_f32 v145, v6, v7\n\t" \
"v_pk_mul_f32 v[12:13], v[12:13], v[238:239] op_sel_hi:[1,0]\n\t" \
"v_pk_mul_f32 v[14:15], v[14:15], v[238:239] op_sel_hi:[1,0]\n\t" \
"v_cvt_pk_bf16_f32 v146, v12, v13\n\t" \
"v_cvt_pk_bf16_f32 v147, v14, v15\n\t" \
"ds_write_b128 v242, v[144:147] offset:1024\n\t" \
"v_pk_mul_f32 v[20:21], v[20:21], v[238:239] op_sel_hi:[1,0]\n\t" \
"v_pk_mul_f32 v[22:23], v[22:23], v[238:239] op_sel_hi:[1,0]\n\t" \
"v_cvt_pk_bf16_f32 v148, v20, v21\n\t" \
"v_cvt_pk_bf16_f32 v149, v22, v23\n\t" \
"v_pk_mul_f32 v[28:29], v[28:29], v[238:239] op_sel_hi:[1,0]\n\t" \
"v_pk_mul_f32 v[30:31], v[30:31], v[238:239] op_sel_hi:[1,0]\n\t" \
"v_cvt_pk_bf16_f32 v150, v28, v29\n\t" \
"v_cvt_pk_bf16_f32 v151, v30, v31\n\t" \
"ds_write_b128 v242, v[148:151] offset:9216\n\t" \
"v_mov_b32 v232, 1\n\t" \
"s_waitcnt lgkmcnt(0)\n\t" \
"ds_write_b32 v243, v232\n\t" \
"v_pk_mul_f32 v[36:37], v[36:37], v[238:239] op_sel_hi:[1,0]\n\t" \
"v_pk_mul_f32 v[38:39], v[38:39], v[238:239] op_sel_hi:[1,0]\n\t" \
"v_cvt_pk_bf16_f32 v152, v36, v37\n\t" \
"v_cvt_pk_bf16_f32 v153, v38, v39\n\t" \
"v_pk_mul_f32 v[44:45], v[44:45], v[238:239] op_sel_hi:[1,0]\n\t" \
"v_pk_mul_f32 v[46:47], v[46:47], v[238:239] op_sel_hi:[1,0]\n\t" \
"v_cvt_pk_bf16_f32 v154, v44, v45\n\t" \
"v_cvt_pk_bf16_f32 v155, v46, v47\n\t" \
"ds_write_b128 v241, v[152:155] offset:33792\n\t" \
"v_pk_mul_f32 v[52:53], v[52:53], v[238:239] op_sel_hi:[1,0]\n\t" \
"v_pk_mul_f32 v[54:55], v[54:55], v[238:239] op_sel_hi:[1,0]\n\t" \
"v_cvt_pk_bf16_f32 v156, v52, v53\n\t" \
"v_cvt_pk_bf16_f32 v157, v54, v55\n\t" \
"v_pk_mul_f32 v[60:61], v[60:61], v[238:239] op_sel_hi:[1,0]\n\t" \
"v_pk_mul_f32 v[62:63], v[62:63], v[238:239] op_sel_hi:[1,0]\n\t" \
"v_cvt_pk_bf16_f32 v158, v60, v61\n\t" \
"v_cvt_pk_bf16_f32 v159, v62, v63\n\t" \
"ds_write_b128 v241, v[156:159] offset:41984\n\t" \
"v_mov_b32 v232, 2\n\t" \
"s_waitcnt lgkmcnt(0)\n\t" \
"ds_write_b32 v243, v232\n\t" \
"s_branch L_atail_%=\n\t" \
"L_rare2_%=:\n\t" \
"s_nop 7\n\t" \
"v_maximum3_f32 v236, v192, v193, v194\n\t" \
"v_maximum3_f32 v236, v236, v195, v200\n\t" \
"v_maximum3_f32 v236, v236, v201, v202\n\t" \
"v_maximum3_f32 v236, v236, v203, v203\n\t" \
"v_mov_b32 v237, v236\n\t" \
"s_nop 1\n\t" \
"v_permlane16_swap_b32 v236, v237\n\t" \
"v_maximum3_f32 v236, v236, v237, v237\n\t" \
"v_mov_b32 v237, v236\n\t" \
"s_nop 1\n\t" \
"v_permlane32_swap_b32 v236, v237\n\t" \
"v_maximum3_f32 v236, v236, v237, 0\n\t" \
"v_sub_f32 v192, v192, v236\n\t" \
"v_sub_f32 v193, v193, v236\n\t" \
"v_sub_f32 v194, v194, v236\n\t" \
"v_sub_f32 v195, v195, v236\n\t" \
"v_sub_f32 v200, v200, v236\n\t" \
"v_sub_f32 v201, v201, v236\n\t" \
"v_sub_f32 v202, v202, v236\n\t" \
"v_sub_f32 v203, v203, v236\n\t" \
"v_sub_f32 v72, v72, v236\n\t" \
"v_sub_f32 v73, v73, v236\n\t" \
"v_sub_f32 v74, v74, v236\n\t" \
"v_sub_f32 v75, v75, v236\n\t" \
"v_exp_f32 v236, -v236\n\t" \
"s_nop 0\n\t" \
"v_mul_f32 v64, v64, v236\n\t" \
"v_mul_f32 v65, v65, v236\n\t" \
"v_mul_f32 v66, v66, v236\n\t" \
"v_mul_f32 v67, v67, v236\n\t" \
"v_mul_f32 v0, v0, v236\n\t" \
"v_mul_f32 v1, v1, v236\n\t" \
"v_mul_f32 v2, v2, v236\n\t" \
"v_mul_f32 v3, v3, v236\n\t" \
"v_mul_f32 v8, v8, v236\n\t" \
"v_mul_f32 v9, v9, v236\n\t" \
"v_mul_f32 v10, v10, v236\n\t" \
"v_mul_f32 v11, v11, v236\n\t" \
"v_mul_f32 v16, v16, v236\n\t" \
"v_mul_f32 v17, v17, v236\n\t" \
"v_mul_f32 v18, v18, v236\n\t" \
"v_mul_f32 v19, v19, v236\n\t" \
"v_mul_f32 v24, v24, v236\n\t" \
"v_mul_f32 v25, v25, v236\n\t" \
"v_mul_f32 v26, v26, v236\n\t" \
"v_mul_f32 v27, v27, v236\n\t" \
"v_mul_f32 v32, v32, v236\n\t" \
"v_mul_f32 v33, v33, v236\n\t" \
"v_mul_f32 v34, v34, v236\n\t" \
"v_mul_f32 v35, v35, v236\n\t" \
"v_mul_f32 v40, v40, v236\n\t" \
"v_mul_f32 v41, v41, v236\n\t" \
"v_mul_f32 v42, v42, v236\n\t" \
"v_mul_f32 v43, v43, v236\n\t" \
"v_mul_f32 v48, v48, v236\n\t" \
"v_mul_f32 v49, v49, v236\n\t" \
"v_mul_f32 v50, v50, v236\n\t" \
"v_mul_f32 v51, v51, v236\n\t" \
"v_mul_f32 v56, v56, v236\n\t" \
"v_mul_f32 v57, v57, v236\n\t" \
"v_mul_f32 v58, v58, v236\n\t" \
"v_mul_f32 v59, v59, v236\n\t" \
"v_maximum3_f32 v236, v196, v197, v198\n\t" \
"v_maximum3_f32 v236, v236, v199, v204\n\t" \
"v_maximum3_f32 v236, v236, v205, v206\n\t" \
"v_maximum3_f32 v236, v236, v207, v207\n\t" \
"v_mov_b32 v237, v236\n\t" \
"s_nop 1\n\t" \
"v_permlane16_swap_b32 v236, v237\n\t" \
"v_maximum3_f32 v236, v236, v237, v237\n\t" \
"v_mov_b32 v237, v236\n\t" \
"s_nop 1\n\t" \
"v_permlane32_swap_b32 v236, v237\n\t" \
"v_maximum3_f32 v236, v236, v237, 0\n\t" \
"v_sub_f32 v196, v196, v236\n\t" \
"v_sub_f32 v197, v197, v236\n\t" \
"v_sub_f32 v198, v198, v236\n\t" \
"v_sub_f32 v199, v199, v236\n\t" \
"v_sub_f32 v204, v204, v236\n\t" \
"v_sub_f32 v205, v205, v236\n\t" \
"v_sub_f32 v206, v206, v236\n\t" \
"v_sub_f32 v207, v207, v236\n\t" \
"v_sub_f32 v76, v76, v236\n\t" \
"v_sub_f32 v77, v77, v236\n\t" \
"v_sub_f32 v78, v78, v236\n\t" \
"v_sub_f32 v79, v79, v236\n\t" \
"v_exp_f32 v236, -v236\n\t" \
"s_nop 0\n\t" \
"v_mul_f32 v68, v68, v236\n\t" \
"v_mul_f32 v69, v69, v236\n\t" \
"v_mul_f32 v70, v70, v236\n\t" \
"v_mul_f32 v71, v71, v236\n\t" \
"v_mul_f32 v4, v4, v236\n\t" \
"v_mul_f32 v5, v5, v236\n\t" \
"v_mul_f32 v6, v6, v236\n\t" \
"v_mul_f32 v7, v7, v236\n\t" \
"v_mul_f32 v12, v12, v236\n\t" \
"v_mul_f32 v13, v13, v236\n\t" \
"v_mul_f32 v14, v14, v236\n\t" \
"v_mul_f32 v15, v15, v236\n\t" \
"v_mul_f32 v20, v20, v236\n\t" \
"v_mul_f32 v21, v21, v236\n\t" \
"v_mul_f32 v22, v22, v236\n\t" \
"v_mul_f32 v23, v23, v236\n\t" \
"v_mul_f32 v28, v28, v236\n\t" \
"v_mul_f32 v29, v29, v236\n\t" \
"v_mul_f32 v30, v30, v236\n\t" \
"v_mul_f32 v31, v31, v236\n\t" \
"v_mul_f32 v36, v36, v236\n\t" \
"v_mul_f32 v37, v37, v236\n\t" \
"v_mul_f32 v38, v38, v236\n\t" \
"v_mul_f32 v39, v39, v236\n\t" \
"v_mul_f32 v44, v44, v236\n\t" \
"v_mul_f32 v45, v45, v236\n\t" \
"v_mul_f32 v46, v46, v236\n\t" \
"v_mul_f32 v47, v47, v236\n\t" \
"v_mul_f32 v52, v52, v236\n\t" \
"v_mul_f32 v53, v53, v236\n\t" \
"v_mul_f32 v54, v54, v236\n\t" \
"v_mul_f32 v55, v55, v236\n\t" \
"v_mul_f32 v60, v60, v236\n\t" \
"v_mul_f32 v61, v61, v236\n\t" \
"v_mul_f32 v62, v62, v236\n\t" \
"v_mul_f32 v63, v63, v236\n\t" \
"s_nop 1\n\t" \
"s_branch L_back2_%=\n\t" \
"L_rare3_%=:\n\t" \
"s_nop 7\n\t" \
"v_maximum3_f32 v236, v176, v177, v178\n\t" \
"v_maximum3_f32 v236, v236, v179, v184\n\t" \
"v_maximum3_f32 v236, v236, v185, v186\n\t" \
"v_maximum3_f32 v236, v236, v187, v187\n\t" \
"v_mov_b32 v237, v236\n\t" \
"s_nop 1\n\t" \
"v_permlane16_swap_b32 v236, v237\n\t" \
"v_maximum3_f32 v236, v236, v237, v237\n\t" \
"v_mov_b32 v237, v236\n\t" \
"s_nop 1\n\t" \
"v_permlane32_swap_b32 v236, v237\n\t" \
"v_maximum3_f32 v236, v236, v237, 0\n\t" \
"v_sub_f32 v176, v176, v236\n\t" \
"v_sub_f32 v177, v177, v236\n\t" \
"v_sub_f32 v178, v178, v236\n\t" \
"v_sub_f32 v179, v179, v236\n\t" \
"v_sub_f32 v184, v184, v236\n\t" \
"v_sub_f32 v185, v185, v236\n\t" \
"v_sub_f32 v186, v186, v236\n\t" \
"v_sub_f32 v187, v187, v236\n\t" \
"v_sub_f32 v72, v72, v236\n\t" \
"v_sub_f32 v73, v73, v236\n\t" \
"v_sub_f32 v74, v74, v236\n\t" \
"v_sub_f32 v75, v75, v236\n\t" \
"v_exp_f32 v236, -v236\n\t" \
"s_nop 0\n\t" \
"v_mul_f32 v64, v64, v236\n\t" \
"v_mul_f32 v65, v65, v236\n\t" \
"v_mul_f32 v66, v66, v236\n\t" \
"v_mul_f32 v67, v67, v236\n\t" \
"v_mul_f32 v0, v0, v236\n\t" \
"v_mul_f32 v1, v1, v236\n\t" \
"v_mul_f32 v2, v2, v236\n\t" \
"v_mul_f32 v3, v3, v236\n\t" \
"v_mul_f32 v8, v8, v236\n\t" \
"v_mul_f32 v9, v9, v236\n\t" \
"v_mul_f32 v10, v10, v236\n\t" \
"v_mul_f32 v11, v11, v236\n\t" \
"v_mul_f32 v16, v16, v236\n\t" \
"v_mul_f32 v17, v17, v236\n\t" \
"v_mul_f32 v18, v18, v236\n\t" \
"v_mul_f32 v19, v19, v236\n\t" \
"v_mul_f32 v24, v24, v236\n\t" \
"v_mul_f32 v25, v25, v236\n\t" \
"v_mul_f32 v26, v26, v236\n\t" \
"v_mul_f32 v27, v27, v236\n\t" \
"v_mul_f32 v32, v32, v236\n\t" \
"v_mul_f32 v33, v33, v236\n\t" \
"v_mul_f32 v34, v34, v236\n\t" \
"v_mul_f32 v35, v35, v236\n\t" \
"v_mul_f32 v40, v40, v236\n\t" \
"v_mul_f32 v41, v41, v236\n\t" \
"v_mul_f32 v42, v42, v236\n\t" \
"v_mul_f32 v43, v43, v236\n\t" \
"v_mul_f32 v48, v48, v236\n\t" \
"v_mul_f32 v49, v49, v236\n\t" \
"v_mul_f32 v50, v50, v236\n\t" \
"v_mul_f32 v51, v51, v236\n\t" \
"v_mul_f32 v56, v56, v236\n\t" \
"v_mul_f32 v57, v57, v236\n\t" \
"v_mul_f32 v58, v58, v236\n\t" \
"v_mul_f32 v59, v59, v236\n\t" \
"v_maximum3_f32 v236, v180, v181, v182\n\t" \
"v_maximum3_f32 v236, v236, v183, v188\n\t" \
"v_maximum3_f32 v236, v236, v189, v190\n\t" \
"v_maximum3_f32 v236, v236, v191, v191\n\t" \
"v_mov_b32 v237, v236\n\t" \
"s_nop 1\n\t" \
"v_permlane16_swap_b32 v236, v237\n\t" \
"v_maximum3_f32 v236, v236, v237, v237\n\t" \
"v_mov_b32 v237, v236\n\t" \
"s_nop 1\n\t" \
"v_permlane32_swap_b32 v236, v237\n\t" \
"v_maximum3_f32 v236, v236, v237, 0\n\t" \
"v_sub_f32 v180, v180, v236\n\t" \
"v_sub_f32 v181, v181, v236\n\t" \
"v_sub_f32 v182, v182, v236\n\t" \
"v_sub_f32 v183, v183, v236\n\t" \
"v_sub_f32 v188, v188, v236\n\t" \
"v_sub_f32 v189, v189, v236\n\t" \
"v_sub_f32 v190, v190, v236\n\t" \
"v_sub_f32 v191, v191, v236\n\t" \
"v_sub_f32 v76, v76, v236\n\t" \
"v_sub_f32 v77, v77, v236\n\t" \
"v_sub_f32 v78, v78, v236\n\t" \
"v_sub_f32 v79, v79, v236\n\t" \
"v_exp_f32 v236, -v236\n\t" \
"s_nop 0\n\t" \
"v_mul_f32 v68, v68, v236\n\t" \
"v_mul_f32 v69, v69, v236\n\t" \
"v_mul_f32 v70, v70, v236\n\t" \
"v_mul_f32 v71, v71, v236\n\t" \
"v_mul_f32 v4, v4, v236\n\t" \
"v_mul_f32 v5, v5, v236\n\t" \
"v_mul_f32 v6, v6, v236\n\t" \
"v_mul_f32 v7, v7, v236\n\t" \
"v_mul_f32 v12, v12, v236\n\t" \
"v_mul_f32 v13, v13, v236\n\t" \
"v_mul_f32 v14, v14, v236\n\t" \
"v_mul_f32 v15, v15, v236\n\t" \
"v_mul_f32 v20, v20, v236\n\t" \
"v_mul_f32 v21, v21, v236\n\t" \
"v_mul_f32 v22, v22, v236\n\t" \
"v_mul_f32 v23, v23, v236\n\t" \
"v_mul_f32 v28, v28, v236\n\t" \
"v_mul_f32 v29, v29, v236\n\t" \
"v_mul_f32 v30, v30, v236\n\t" \
"v_mul_f32 v31, v31, v236\n\t" \
"v_mul_f32 v36, v36, v236\n\t" \
"v_mul_f32 v37, v37, v236\n\t" \
"v_mul_f32 v38, v38, v236\n\t" \
"v_mul_f32 v39, v39, v236\n\t" \
"v_mul_f32 v44, v44, v236\n\t" \
"v_mul_f32 v45, v45, v236\n\t" \
"v_mul_f32 v46, v46, v236\n\t" \
"v_mul_f32 v47, v47, v236\n\t" \
"v_mul_f32 v52, v52, v236\n\t" \
"v_mul_f32 v53, v53, v236\n\t" \
"v_mul_f32 v54, v54, v236\n\t" \
"v_mul_f32 v55, v55, v236\n\t" \
"v_mul_f32 v60, v60, v236\n\t" \
"v_mul_f32 v61, v61, v236\n\t" \
"v_mul_f32 v62, v62, v236\n\t" \
"v_mul_f32 v63, v63, v236\n\t" \
"s_nop 1\n\t" \
"s_branch L_back3_%=\n\t" \
"L_rare4_%=:\n\t" \
"s_nop 7\n\t" \
"v_maximum3_f32 v236, v192, v193, v194\n\t" \
"v_maximum3_f32 v236, v236, v195, v200\n\t" \
"v_maximum3_f32 v236, v236, v201, v202\n\t" \
"v_maximum3_f32 v236, v236, v203, v203\n\t" \
"v_mov_b32 v237, v236\n\t" \
"s_nop 1\n\t" \
"v_permlane16_swap_b32 v236, v237\n\t" \
"v_maximum3_f32 v236, v236, v237, v237\n\t" \
"v_mov_b32 v237, v236\n\t" \
"s_nop 1\n\t" \
"v_permlane32_swap_b32 v236, v237\n\t" \
"v_maximum3_f32 v236, v236, v237, 0\n\t" \
"v_sub_f32 v192, v192, v236\n\t" \
"v_sub_f32 v193, v193, v236\n\t" \
"v_sub_f32 v194, v194, v236\n\t" \
"v_sub_f32 v195, v195, v236\n\t" \
"v_sub_f32 v200, v200, v236\n\t" \
"v_sub_f32 v201, v201, v236\n\t" \
"v_sub_f32 v202, v202, v236\n\t" \
"v_sub_f32 v203, v203, v236\n\t" \
"v_sub_f32 v72, v72, v236\n\t" \
"v_sub_f32 v73, v73, v236\n\t" \
"v_sub_f32 v74, v74, v236\n\t" \
"v_sub_f32 v75, v75, v236\n\t" \
"v_exp_f32 v236, -v236\n\t" \
"s_nop 0\n\t" \
"v_mul_f32 v64, v64, v236\n\t" \
"v_mul_f32 v65, v65, v236\n\t" \
"v_mul_f32 v66, v66, v236\n\t" \
"v_mul_f32 v67, v67, v236\n\t" \
"v_mul_f32 v0, v0, v236\n\t" \
"v_mul_f32 v1, v1, v236\n\t" \
"v_mul_f32 v2, v2, v236\n\t" \
"v_mul_f32 v3, v3, v236\n\t" \
"v_mul_f32 v8, v8, v236\n\t" \
"v_mul_f32 v9, v9, v236\n\t" \
"v_mul_f32 v10, v10, v236\n\t" \
"v_mul_f32 v11, v11, v236\n\t" \
"v_mul_f32 v16, v16, v236\n\t" \
"v_mul_f32 v17, v17, v236\n\t" \
"v_mul_f32 v18, v18, v236\n\t" \
"v_mul_f32 v19, v19, v236\n\t" \
"v_mul_f32 v24, v24, v236\n\t" \
"v_mul_f32 v25, v25, v236\n\t" \
"v_mul_f32 v26, v26, v236\n\t" \
"v_mul_f32 v27, v27, v236\n\t" \
"v_mul_f32 v32, v32, v236\n\t" \
"v_mul_f32 v33, v33, v236\n\t" \
"v_mul_f32 v34, v34, v236\n\t" \
"v_mul_f32 v35, v35, v236\n\t" \
"v_mul_f32 v40, v40, v236\n\t" \
"v_mul_f32 v41, v41, v236\n\t" \
"v_mul_f32 v42, v42, v236\n\t" \
"v_mul_f32 v43, v43, v236\n\t" \
"v_mul_f32 v48, v48, v236\n\t" \
"v_mul_f32 v49, v49, v236\n\t" \
"v_mul_f32 v50, v50, v236\n\t" \
"v_mul_f32 v51, v51, v236\n\t" \
"v_mul_f32 v56, v56, v236\n\t" \
"v_mul_f32 v57, v57, v236\n\t" \
"v_mul_f32 v58, v58, v236\n\t" \
"v_mul_f32 v59, v59, v236\n\t" \
"v_maximum3_f32 v236, v196, v197, v198\n\t" \
"v_maximum3_f32 v236, v236, v199, v204\n\t" \
"v_maximum3_f32 v236, v236, v205, v206\n\t" \
"v_maximum3_f32 v236, v236, v207, v207\n\t" \
"v_mov_b32 v237, v236\n\t" \
"s_nop 1\n\t" \
"v_permlane16_swap_b32 v236, v237\n\t" \
"v_maximum3_f32 v236, v236, v237, v237\n\t" \
"v_mov_b32 v237, v236\n\t" \
"s_nop 1\n\t" \
"v_permlane32_swap_b32 v236, v237\n\t" \
"v_maximum3_f32 v236, v236, v237, 0\n\t" \
"v_sub_f32 v196, v196, v236\n\t" \
"v_sub_f32 v197, v197, v236\n\t" \
"v_sub_f32 v198, v198, v236\n\t" \
"v_sub_f32 v199, v199, v236\n\t" \
"v_sub_f32 v204, v204, v236\n\t" \
"v_sub_f32 v205, v205, v236\n\t" \
"v_sub_f32 v206, v206, v236\n\t" \
"v_sub_f32 v207, v207, v236\n\t" \
"v_sub_f32 v76, v76, v236\n\t" \
"v_sub_f32 v77, v77, v236\n\t" \
"v_sub_f32 v78, v78, v236\n\t" \
"v_sub_f32 v79, v79, v236\n\t" \
"v_exp_f32 v236, -v236\n\t" \
"s_nop 0\n\t" \
"v_mul_f32 v68, v68, v236\n\t" \
"v_mul_f32 v69, v69, v236\n\t" \
"v_mul_f32 v70, v70, v236\n\t" \
"v_mul_f32 v71, v71, v236\n\t" \
"v_mul_f32 v4, v4, v236\n\t" \
"v_mul_f32 v5, v5, v236\n\t" \
"v_mul_f32 v6, v6, v236\n\t" \
"v_mul_f32 v7, v7, v236\n\t" \
"v_mul_f32 v12, v12, v236\n\t" \
"v_mul_f32 v13, v13, v236\n\t" \
"v_mul_f32 v14, v14, v236\n\t" \
"v_mul_f32 v15, v15, v236\n\t" \
"v_mul_f32 v20, v20, v236\n\t" \
"v_mul_f32 v21, v21, v236\n\t" \
"v_mul_f32 v22, v22, v236\n\t" \
"v_mul_f32 v23, v23, v236\n\t" \
"v_mul_f32 v28, v28, v236\n\t" \
"v_mul_f32 v29, v29, v236\n\t" \
"v_mul_f32 v30, v30, v236\n\t" \
"v_mul_f32 v31, v31, v236\n\t" \
"v_mul_f32 v36, v36, v236\n\t" \
"v_mul_f32 v37, v37, v236\n\t" \
"v_mul_f32 v38, v38, v236\n\t" \
"v_mul_f32 v39, v39, v236\n\t" \
"v_mul_f32 v44, v44, v236\n\t" \
"v_mul_f32 v45, v45, v236\n\t" \
"v_mul_f32 v46, v46, v236\n\t" \
"v_mul_f32 v47, v47, v236\n\t" \
"v_mul_f32 v52, v52, v236\n\t" \
"v_mul_f32 v53, v53, v236\n\t" \
"v_mul_f32 v54, v54, v236\n\t" \
"v_mul_f32 v55, v55, v236\n\t" \
"v_mul_f32 v60, v60, v236\n\t" \
"v_mul_f32 v61, v61, v236\n\t" \
"v_mul_f32 v62, v62, v236\n\t" \
"v_mul_f32 v63, v63, v236\n\t" \
"s_nop 1\n\t" \
"s_branch L_back4_%=\n\t" \
"L_item1_%=:\n\t" \
"v_mov_b32 v72, 0\n\t" \
"v_mov_b32 v73, 0\n\t" \
"v_mov_b32 v74, 0\n\t" \
"v_mov_b32 v75, 0\n\t" \
"s_lshl_b32 s60, s32, 13\n\t" \
"s_lshl_b32 s61, s68, 15\n\t" \
"s_add_u32 s60, s60, s61\n\t" \
"s_sub_u32 s60, s60, s65\n\t" \
"v_add_u32 v240, s60, v220\n\t" \
"v_add_u32 v241, s60, v221\n\t" \
"v_add_u32 v242, s60, v222\n\t" \
"v_add_u32 v243, s60, v223\n\t" \
"s_barrier\n\t" \
"ds_read_b128 v[80:83], v240 offset:0\n\t" \
"ds_read_b128 v[84:87], v241 offset:0\n\t" \
"ds_read_b128 v[88:91], v242 offset:0\n\t" \
"ds_read_b128 v[92:95], v243 offset:0\n\t" \
"ds_read_b128 v[112:115], v220 offset:8192\n\t" \
"ds_read_b128 v[116:119], v221 offset:8192\n\t" \
"ds_read_b128 v[120:123], v222 offset:8192\n\t" \
"ds_read_b128 v[124:127], v223 offset:8192\n\t" \
"ds_read_b128 v[128:131], v220 offset:12288\n\t" \
"ds_read_b128 v[132:135], v221 offset:12288\n\t" \
"ds_read_b128 v[136:139], v222 offset:12288\n\t" \
"ds_read_b128 v[140:143], v223 offset:12288\n\t" \
"v_mov_b32 v64, 0\n\t" \
"v_mov_b32 v65, 0\n\t" \
"v_mov_b32 v66, 0\n\t" \
"v_mov_b32 v67, 0\n\t" \
"v_mov_b32 v0, 0\n\t" \
"v_mov_b32 v1, 0\n\t" \
"v_mov_b32 v2, 0\n\t" \
"v_mov_b32 v3, 0\n\t" \
"v_mov_b32 v8, 0\n\t" \
"v_mov_b32 v9, 0\n\t" \
"v_mov_b32 v10, 0\n\t" \
"v_mov_b32 v11, 0\n\t" \
"v_mov_b32 v16, 0\n\t" \
"v_mov_b32 v17, 0\n\t" \
"v_mov_b32 v18, 0\n\t" \
"v_mov_b32 v19, 0\n\t" \
"v_mov_b32 v24, 0\n\t" \
"v_mov_b32 v25, 0\n\t" \
"v_mov_b32 v26, 0\n\t" \
"v_mov_b32 v27, 0\n\t" \
"v_mov_b32 v32, 0\n\t" \
"v_mov_b32 v33, 0\n\t" \
"v_mov_b32 v34, 0\n\t" \
"v_mov_b32 v35, 0\n\t" \
"v_mov_b32 v40, 0\n\t" \
"v_mov_b32 v41, 0\n\t" \
"v_mov_b32 v42, 0\n\t" \
"v_mov_b32 v43, 0\n\t" \
"v_mov_b32 v48, 0\n\t" \
"v_mov_b32 v49, 0\n\t" \
"v_mov_b32 v50, 0\n\t" \
"v_mov_b32 v51, 0\n\t" \
"v_mov_b32 v56, 0\n\t" \
"v_mov_b32 v57, 0\n\t" \
"v_mov_b32 v58, 0\n\t" \
"v_mov_b32 v59, 0\n\t" \
"s_waitcnt lgkmcnt(7)\n\t" \
"v_mfma_f32_16x16x32_bf16 v[176:179], v[112:115], v[80:83], v[72:75]\n\t" \
"s_waitcnt lgkmcnt(6)\n\t" \
"v_mfma_f32_16x16x32_bf16 v[176:179], v[116:119], v[84:87], v[176:179]\n\t" \
"s_waitcnt lgkmcnt(5)\n\t" \
"v_mfma_f32_16x16x32_bf16 v[176:179], v[120:123], v[88:91], v[176:179]\n\t" \
"s_waitcnt lgkmcnt(4)\n\t" \
"v_mfma_f32_16x16x32_bf16 v[176:179], v[124:127], v[92:95], v[176:179]\n\t" \
"s_waitcnt lgkmcnt(3)\n\t" \
"v_mfma_f32_16x16x32_bf16 v[184:187], v[128:131], v[80:83], v[72:75]\n\t" \
"s_waitcnt lgkmcnt(2)\n\t" \
"v_mfma_f32_16x16x32_bf16 v[184:187], v[132:135], v[84:87], v[184:187]\n\t" \
"s_waitcnt lgkmcnt(1)\n\t" \
"v_mfma_f32_16x16x32_bf16 v[184:187], v[136:139], v[88:91], v[184:187]\n\t" \
"s_waitcnt lgkmcnt(0)\n\t" \
"v_mfma_f32_16x16x32_bf16 v[184:187], v[140:143], v[92:95], v[184:187]\n\t" \
"s_cmp_eq_u32 s68, 0\n\t" \
"s_cbranch_scc1 L_no15_%=\n\t" \
"s_lshl_b32 s61, s32, 11\n\t" \
"v_add_u32 v232, s61, v250\n\t" \
"ds_write_b128 v232, v[192:195] offset:0\n\t" \
"ds_write_b128 v232, v[196:199] offset:8192\n\t" \
"ds_write_b128 v232, v[200:203] offset:16384\n\t" \
"ds_write_b128 v232, v[204:207] offset:24576\n\t" \
"s_waitcnt lgkmcnt(0)\n\t" \
"L_no15_%=:\n\t" \
"s_barrier\n\t" \
"s_cmp_eq_u32 s65, 0x10000\n\t" \
"s_cselect_b32 s67, s65, 0xffff8000\n\t" \
"s_add_u32 s65, s65, s67\n\t" \
"v_add_u32 v220, s67, v220\n\t" \
"v_add_u32 v221, s67, v221\n\t" \
"v_add_u32 v222, s67, v222\n\t" \
"v_add_u32 v223, s67, v223\n\t" \
"s_cmp_eq_u32 s66, 0x10000\n\t" \
"s_cselect_b32 s67, s66, 0xffff8000\n\t" \
"s_add_u32 s66, s66, s67\n\t" \
"v_add_u32 v224, s67, v224\n\t" \
"ds_read_b128 v[112:115], v220 offset:0\n\t" \
"ds_read_b128 v[116:119], v221 offset:0\n\t" \
"ds_read_b64_tr_b16 v[144:145], v224 offset:0\n\t" \
"ds_read_b64_tr_b16 v[146:147], v224 offset:4096\n\t" \
"v_add_u32 v225, s67, v225\n\t" \
"ds_read_b128 v[120:123], v222 offset:0\n\t" \
"ds_read_b64_tr_b16 v[148:149], v225 offset:0\n\t" \
"ds_read_b64_tr_b16 v[150:151], v225 offset:4096\n\t" \
"v_add_u32 v226, s67, v226\n\t" \
"ds_read_b128 v[124:127], v223 offset:0\n\t" \
"ds_read_b64_tr_b16 v[152:153], v226 offset:0\n\t" \
"ds_read_b64_tr_b16 v[154:155], v226 offset:4096\n\t" \
"v_add_u32 v227, s67, v227\n\t" \
"ds_read_b128 v[128:131], v220 offset:4096\n\t" \
"ds_read_b64_tr_b16 v[156:157], v227 offset:0\n\t" \
"ds_read_b64_tr_b16 v[158:159], v227 offset:4096\n\t" \
"v_add_u32 v228, s67, v228\n\t" \
"ds_read_b128 v[132:135], v221 offset:4096\n\t" \
"ds_read_b64_tr_b16 v[160:161], v228 offset:0\n\t" \
"ds_read_b64_tr_b16 v[162:163], v228 offset:4096\n\t" \
"v_add_u32 v229, s67, v229\n\t" \
"ds_read_b128 v[136:139], v222 offset:4096\n\t" \
"ds_read_b64_tr_b16 v[164:165], v229 offset:0\n\t" \
"ds_read_b64_tr_b16 v[166:167], v229 offset:4096\n\t" \
"v_add_u32 v230, s67, v230\n\t" \
"ds_read_b128 v[140:143], v223 offset:4096\n\t" \
"ds_read_b64_tr_b16 v[168:169], v230 offset:0\n\t" \
"ds_read_b64_tr_b16 v[170:171], v230 offset:4096\n\t" \
"v_add_u32 v231, s67, v231\n\t" \
"ds_read_b64_tr_b16 v[172:173], v231 offset:0\n\t" \
"ds_read_b64_tr_b16 v[174:175], v231 offset:4096\n\t" \
"v_maximum3_f32 v236, v176, v177, v178\n\t" \
"v_maximum3_f32 v236, v236, v179, v184\n\t" \
"v_maximum3_f32 v236, v236, v185, v186\n\t" \
"v_maximum3_f32 v236, v236, v187, v187\n\t" \
"v_mov_b32 v237, v236\n\t" \
"s_nop 1\n\t" \
"v_permlane16_swap_b32 v236, v237\n\t" \
"v_maximum3_f32 v236, v236, v237, v237\n\t" \
"v_mov_b32 v237, v236\n\t" \
"s_nop 1\n\t" \
"v_permlane32_swap_b32 v236, v237\n\t" \
"v_maximum3_f32 v236, v236, v237, v237\n\t" \
"v_sub_f32 v176, v176, v236\n\t" \
"v_sub_f32 v177, v177, v236\n\t" \
"v_sub_f32 v178, v178, v236\n\t" \
"v_sub_f32 v179, v179, v236\n\t" \
"v_sub_f32 v184, v184, v236\n\t" \
"v_sub_f32 v185, v185, v236\n\t" \
"v_sub_f32 v186, v186, v236\n\t" \
"v_sub_f32 v187, v187, v236\n\t" \
"v_sub_f32 v72, v72, v236\n\t" \
"v_sub_f32 v73, v73, v236\n\t" \
"v_sub_f32 v74, v74, v236\n\t" \
"v_sub_f32 v75, v75, v236\n\t" \
"s_mov_b32 s64, 15\n\t" \
"L_loop1_%=:\n\t" \
"s_waitcnt lgkmcnt(14)\n\t" \
"v_exp_f32 v232, v176\n\t" \
"v_mfma_f32_16x16x32_bf16 v[192:195], v[112:115], v[80:83], v[72:75]\n\t" \
"v_exp_f32 v233, v177\n\t" \
"v_mfma_f32_16x16x32_bf16 v[192:195], v[116:119], v[84:87], v[192:195]\n\t" \
"v_exp_f32 v234, v178\n\t" \
"v_cvt_pk_bf16_f32 v208, v232, v233\n\t" \
"v_mfma_f32_16x16x32_bf16 v[192:195], v[120:123], v[88:91], v[192:195]\n\t" \
"s_waitcnt lgkmcnt(0)\n\t" \
"v_exp_f32 v235, v179\n\t" \
"v_mfma_f32_16x16x32_bf16 v[192:195], v[124:127], v[92:95], v[192:195]\n\t" \
"v_exp_f32 v232, v184\n\t" \
"v_cvt_pk_bf16_f32 v209, v234, v235\n\t" \
"v_mfma_f32_16x16x32_bf16 v[200:203], v[128:131], v[80:83], v[72:75]\n\t" \
"v_exp_f32 v233, v185\n\t" \
"v_mfma_f32_16x16x32_bf16 v[200:203], v[132:135], v[84:87], v[200:203]\n\t" \
"v_exp_f32 v234, v186\n\t" \
"v_cvt_pk_bf16_f32 v210, v232, v233\n\t" \
"v_mfma_f32_16x16x32_bf16 v[200:203], v[136:139], v[88:91], v[200:203]\n\t" \
"v_exp_f32 v235, v187\n\t" \
"v_mfma_f32_16x16x32_bf16 v[200:203], v[140:143], v[92:95], v[200:203]\n\t" \
"v_cvt_pk_bf16_f32 v211, v234, v235\n\t" \
"s_cmp_eq_u32 s65, 0x10000\n\t" \
"s_cselect_b32 s67, s65, 0xffff8000\n\t" \
"s_add_u32 s65, s65, s67\n\t" \
"v_mfma_f32_16x16x32_bf16 v[0:3], v[144:147], v[208:211], v[0:3]\n\t" \
"ds_read_b128 v[112:115], v220 offset:8192\n\t" \
"v_mfma_f32_16x16x32_bf16 v[8:11], v[148:151], v[208:211], v[8:11]\n\t" \
"ds_read_b128 v[116:119], v221 offset:8192\n\t" \
"ds_read_b64_tr_b16 v[144:145], v224 offset:8192\n\t" \
"ds_read_b64_tr_b16 v[146:147], v224 offset:12288\n\t" \
"v_maximum3_f32 v236, v192, v193, v194\n\t" \
"v_mfma_f32_16x16x32_bf16 v[16:19], v[152:155], v[208:211], v[16:19]\n\t" \
"ds_read_b128 v[120:123], v222 offset:8192\n\t" \
"ds_read_b64_tr_b16 v[148:149], v225 offset:8192\n\t" \
"ds_read_b64_tr_b16 v[150:151], v225 offset:12288\n\t" \
"v_maximum3_f32 v236, v236, v195, v200\n\t" \
"v_mfma_f32_16x16x32_bf16 v[24:27], v[156:159], v[208:211], v[24:27]\n\t" \
"ds_read_b128 v[124:127], v223 offset:8192\n\t" \
"ds_read_b64_tr_b16 v[152:153], v226 offset:8192\n\t" \
"ds_read_b64_tr_b16 v[154:155], v226 offset:12288\n\t" \
"v_maximum3_f32 v236, v236, v201, v202\n\t" \
"v_mfma_f32_16x16x32_bf16 v[32:35], v[160:163], v[208:211], v[32:35]\n\t" \
"ds_read_b128 v[128:131], v220 offset:12288\n\t" \
"ds_read_b64_tr_b16 v[156:157], v227 offset:8192\n\t" \
"ds_read_b64_tr_b16 v[158:159], v227 offset:12288\n\t" \
"v_maximum3_f32 v236, v236, v203, v203\n\t" \
"v_mfma_f32_16x16x32_bf16 v[40:43], v[164:167], v[208:211], v[40:43]\n\t" \
"ds_read_b128 v[132:135], v221 offset:12288\n\t" \
"ds_read_b64_tr_b16 v[160:161], v228 offset:8192\n\t" \
"ds_read_b64_tr_b16 v[162:163], v228 offset:12288\n\t" \
"v_add_u32 v220, s67, v220\n\t" \
"v_mfma_f32_16x16x32_bf16 v[48:51], v[168:171], v[208:211], v[48:51]\n\t" \
"ds_read_b128 v[136:139], v222 offset:12288\n\t" \
"ds_read_b64_tr_b16 v[164:165], v229 offset:8192\n\t" \
"ds_read_b64_tr_b16 v[166:167], v229 offset:12288\n\t" \
"v_add_u32 v221, s67, v221\n\t" \
"v_mfma_f32_16x16x32_bf16 v[56:59], v[172:175], v[208:211], v[56:59]\n\t" \
"ds_read_b128 v[140:143], v223 offset:12288\n\t" \
"ds_read_b64_tr_b16 v[168:169], v230 offset:8192\n\t" \
"ds_read_b64_tr_b16 v[170:171], v230 offset:12288\n\t" \
"v_add_u32 v222, s67, v222\n\t" \
"v_mfma_f32_16x16x32_bf16 v[64:67], v[216:219], v[208:211], v[64:67]\n\t" \
"ds_read_b64_tr_b16 v[172:173], v231 offset:8192\n\t" \
"ds_read_b64_tr_b16 v[174:175], v231 offset:12288\n\t" \
"v_add_u32 v223, s67, v223\n\t" \
"v_cmp_lt_f32 vcc, s69, v236\n\t" \
"s_cbranch_vccnz L_rare6_%=\n\t" \
"L_back6_%=:\n\t" \
"s_waitcnt lgkmcnt(14)\n\t" \
"v_exp_f32 v232, v192\n\t" \
"v_mfma_f32_16x16x32_bf16 v[176:179], v[112:115], v[80:83], v[72:75]\n\t" \
"v_exp_f32 v233, v193\n\t" \
"v_mfma_f32_16x16x32_bf16 v[176:179], v[116:119], v[84:87], v[176:179]\n\t" \
"v_exp_f32 v234, v194\n\t" \
"v_cvt_pk_bf16_f32 v208, v232, v233\n\t" \
"v_mfma_f32_16x16x32_bf16 v[176:179], v[120:123], v[88:91], v[176:179]\n\t" \
"s_waitcnt lgkmcnt(0)\n\t" \
"s_barrier\n\t" \
"v_exp_f32 v235, v195\n\t" \
"v_mfma_f32_16x16x32_bf16 v[176:179], v[124:127], v[92:95], v[176:179]\n\t" \
"v_exp_f32 v232, v200\n\t" \
"v_cvt_pk_bf16_f32 v209, v234, v235\n\t" \
"v_mfma_f32_16x16x32_bf16 v[184:187], v[128:131], v[80:83], v[72:75]\n\t" \
"v_exp_f32 v233, v201\n\t" \
"v_mfma_f32_16x16x32_bf16 v[184:187], v[132:135], v[84:87], v[184:187]\n\t" \
"v_exp_f32 v234, v202\n\t" \
"v_cvt_pk_bf16_f32 v210, v232, v233\n\t" \
"v_mfma_f32_16x16x32_bf16 v[184:187], v[136:139], v[88:91], v[184:187]\n\t" \
"v_exp_f32 v235, v203\n\t" \
"v_mfma_f32_16x16x32_bf16 v[184:187], v[140:143], v[92:95], v[184:187]\n\t" \
"v_cvt_pk_bf16_f32 v211, v234, v235\n\t" \
"s_cmp_eq_u32 s66, 0x10000\n\t" \
"s_cselect_b32 s67, s66, 0xffff8000\n\t" \
"s_add_u32 s66, s66, s67\n\t" \
"v_add_u32 v224, s67, v224\n\t" \
"v_mfma_f32_16x16x32_bf16 v[0:3], v[144:147], v[208:211], v[0:3]\n\t" \
"ds_read_b128 v[112:115], v220 offset:0\n\t" \
"v_mfma_f32_16x16x32_bf16 v[8:11], v[148:151], v[208:211], v[8:11]\n\t" \
"ds_read_b128 v[116:119], v221 offset:0\n\t" \
"ds_read_b64_tr_b16 v[144:145], v224 offset:0\n\t" \
"ds_read_b64_tr_b16 v[146:147], v224 offset:4096\n\t" \
"v_maximum3_f32 v236, v176, v177, v178\n\t" \
"v_add_u32 v225, s67, v225\n\t" \
"v_mfma_f32_16x16x32_bf16 v[16:19], v[152:155], v[208:211], v[16:19]\n\t" \
"ds_read_b128 v[120:123], v222 offset:0\n\t" \
"ds_read_b64_tr_b16 v[148:149], v225 offset:0\n\t" \
"ds_read_b64_tr_b16 v[150:151], v225 offset:4096\n\t" \
"v_maximum3_f32 v236, v236, v179, v184\n\t" \
"v_add_u32 v226, s67, v226\n\t" \
"v_mfma_f32_16x16x32_bf16 v[24:27], v[156:159], v[208:211], v[24:27]\n\t" \
"ds_read_b128 v[124:127], v223 offset:0\n\t" \
"ds_read_b64_tr_b16 v[152:153], v226 offset:0\n\t" \
"ds_read_b64_tr_b16 v[154:155], v226 offset:4096\n\t" \
"v_maximum3_f32 v236, v236, v185, v186\n\t" \
"v_add_u32 v227, s67, v227\n\t" \
"v_mfma_f32_16x16x32_bf16 v[32:35], v[160:163], v[208:211], v[32:35]\n\t" \
"ds_read_b128 v[128:131], v220 offset:4096\n\t" \
"ds_read_b64_tr_b16 v[156:157], v227 offset:0\n\t" \
"ds_read_b64_tr_b16 v[158:159], v227 offset:4096\n\t" \
"v_maximum3_f32 v236, v236, v187, v187\n\t" \
"v_add_u32 v228, s67, v228\n\t" \
"v_mfma_f32_16x16x32_bf16 v[40:43], v[164:167], v[208:211], v[40:43]\n\t" \
"ds_read_b128 v[132:135], v221 offset:4096\n\t" \
"ds_read_b64_tr_b16 v[160:161], v228 offset:0\n\t" \
"ds_read_b64_tr_b16 v[162:163], v228 offset:4096\n\t" \
"v_add_u32 v229, s67, v229\n\t" \
"v_mfma_f32_16x16x32_bf16 v[48:51], v[168:171], v[208:211], v[48:51]\n\t" \
"ds_read_b128 v[136:139], v222 offset:4096\n\t" \
"ds_read_b64_tr_b16 v[164:165], v229 offset:0\n\t" \
"ds_read_b64_tr_b16 v[166:167], v229 offset:4096\n\t" \
"v_add_u32 v230, s67, v230\n\t" \
"v_mfma_f32_16x16x32_bf16 v[56:59], v[172:175], v[208:211], v[56:59]\n\t" \
"ds_read_b128 v[140:143], v223 offset:4096\n\t" \
"ds_read_b64_tr_b16 v[168:169], v230 offset:0\n\t" \
"ds_read_b64_tr_b16 v[170:171], v230 offset:4096\n\t" \
"v_add_u32 v231, s67, v231\n\t" \
"v_mfma_f32_16x16x32_bf16 v[64:67], v[216:219], v[208:211], v[64:67]\n\t" \
"ds_read_b64_tr_b16 v[172:173], v231 offset:0\n\t" \
"ds_read_b64_tr_b16 v[174:175], v231 offset:4096\n\t" \
"v_cmp_lt_f32 vcc, s69, v236\n\t" \
"s_cbranch_vccnz L_rare7_%=\n\t" \
"L_back7_%=:\n\t" \
"s_sub_u32 s64, s64, 1\n\t" \
"s_cmp_lg_u32 s64, 0\n\t" \
"s_cbranch_scc1 L_loop1_%=\n\t" \
"s_waitcnt lgkmcnt(14)\n\t" \
"v_exp_f32 v232, v176\n\t" \
"v_mfma_f32_16x16x32_bf16 v[192:195], v[112:115], v[80:83], v[72:75]\n\t" \
"v_exp_f32 v233, v177\n\t" \
"v_mfma_f32_16x16x32_bf16 v[192:195], v[116:119], v[84:87], v[192:195]\n\t" \
"v_exp_f32 v234, v178\n\t" \
"v_cvt_pk_bf16_f32 v208, v232, v233\n\t" \
"v_mfma_f32_16x16x32_bf16 v[192:195], v[120:123], v[88:91], v[192:195]\n\t" \
"s_waitcnt lgkmcnt(0)\n\t" \
"v_exp_f32 v235, v179\n\t" \
"v_mfma_f32_16x16x32_bf16 v[192:195], v[124:127], v[92:95], v[192:195]\n\t" \
"v_exp_f32 v232, v184\n\t" \
"v_cvt_pk_bf16_f32 v209, v234, v235\n\t" \
"v_mfma_f32_16x16x32_bf16 v[200:203], v[128:131], v[80:83], v[72:75]\n\t" \
"v_exp_f32 v233, v185\n\t" \
"v_mfma_f32_16x16x32_bf16 v[200:203], v[132:135], v[84:87], v[200:203]\n\t" \
"v_exp_f32 v234, v186\n\t" \
"v_cvt_pk_bf16_f32 v210, v232, v233\n\t" \
"v_mfma_f32_16x16x32_bf16 v[200:203], v[136:139], v[88:91], v[200:203]\n\t" \
"v_exp_f32 v235, v187\n\t" \
"v_mfma_f32_16x16x32_bf16 v[200:203], v[140:143], v[92:95], v[200:203]\n\t" \
"v_cvt_pk_bf16_f32 v211, v234, v235\n\t" \
"s_nop 1\n\t" \
"v_mfma_f32_16x16x32_bf16 v[0:3], v[144:147], v[208:211], v[0:3]\n\t" \
"v_mfma_f32_16x16x32_bf16 v[8:11], v[148:151], v[208:211], v[8:11]\n\t" \
"ds_read_b64_tr_b16 v[144:145], v224 offset:8192\n\t" \
"ds_read_b64_tr_b16 v[146:147], v224 offset:12288\n\t" \
"v_maximum3_f32 v236, v192, v193, v194\n\t" \
"v_mfma_f32_16x16x32_bf16 v[16:19], v[152:155], v[208:211], v[16:19]\n\t" \
"ds_read_b64_tr_b16 v[148:149], v225 offset:8192\n\t" \
"ds_read_b64_tr_b16 v[150:151], v225 offset:12288\n\t" \
"v_maximum3_f32 v236, v236, v195, v200\n\t" \
"v_mfma_f32_16x16x32_bf16 v[24:27], v[156:159], v[208:211], v[24:27]\n\t" \
"ds_read_b64_tr_b16 v[152:153], v226 offset:8192\n\t" \
"ds_read_b64_tr_b16 v[154:155], v226 offset:12288\n\t" \
"v_maximum3_f32 v236, v236, v201, v202\n\t" \
"v_mfma_f32_16x16x32_bf16 v[32:35], v[160:163], v[208:211], v[32:35]\n\t" \
"ds_read_b64_tr_b16 v[156:157], v227 offset:8192\n\t" \
"ds_read_b64_tr_b16 v[158:159], v227 offset:12288\n\t" \
"v_maximum3_f32 v236, v236, v203, v203\n\t" \
"v_mfma_f32_16x16x32_bf16 v[40:43], v[164:167], v[208:211], v[40:43]\n\t" \
"ds_read_b64_tr_b16 v[160:161], v228 offset:8192\n\t" \
"ds_read_b64_tr_b16 v[162:163], v228 offset:12288\n\t" \
"v_mfma_f32_16x16x32_bf16 v[48:51], v[168:171], v[208:211], v[48:51]\n\t" \
"ds_read_b64_tr_b16 v[164:165], v229 offset:8192\n\t" \
"ds_read_b64_tr_b16 v[166:167], v229 offset:12288\n\t" \
"v_mfma_f32_16x16x32_bf16 v[56:59], v[172:175], v[208:211], v[56:59]\n\t" \
"ds_read_b64_tr_b16 v[168:169], v230 offset:8192\n\t" \
"ds_read_b64_tr_b16 v[170:171], v230 offset:12288\n\t" \
"v_mfma_f32_16x16x32_bf16 v[64:67], v[216:219], v[208:211], v[64:67]\n\t" \
"ds_read_b64_tr_b16 v[172:173], v231 offset:8192\n\t" \
"ds_read_b64_tr_b16 v[174:175], v231 offset:12288\n\t" \
"v_cmp_lt_f32 vcc, s69, v236\n\t" \
"s_cbranch_vccnz L_rare8_%=\n\t" \
"L_back8_%=:\n\t" \
"s_waitcnt lgkmcnt(0)\n\t" \
"s_barrier\n\t" \
"s_cmp_eq_u32 s68, 1\n\t" \
"s_cbranch_scc1 L_last1_%=\n\t" \
"v_exp_f32 v232, v192\n\t" \
"v_exp_f32 v233, v193\n\t" \
"v_exp_f32 v234, v194\n\t" \
"v_cvt_pk_bf16_f32 v208, v232, v233\n\t" \
"v_exp_f32 v235, v195\n\t" \
"v_exp_f32 v232, v200\n\t" \
"v_cvt_pk_bf16_f32 v209, v234, v235\n\t" \
"v_exp_f32 v233, v201\n\t" \
"v_exp_f32 v234, v202\n\t" \
"v_cvt_pk_bf16_f32 v210, v232, v233\n\t" \
"v_exp_f32 v235, v203\n\t" \
"s_nop 0\n\t" \
"v_cvt_pk_bf16_f32 v211, v234, v235\n\t" \
"s_nop 1\n\t" \
"v_mfma_f32_16x16x32_bf16 v[64:67], v[216:219], v[208:211], v[64:67]\n\t" \
"v_mfma_f32_16x16x32_bf16 v[0:3], v[144:147], v[208:211], v[0:3]\n\t" \
"v_mfma_f32_16x16x32_bf16 v[8:11], v[148:151], v[208:211], v[8:11]\n\t" \
"v_mfma_f32_16x16x32_bf16 v[16:19], v[152:155], v[208:211], v[16:19]\n\t" \
"v_mfma_f32_16x16x32_bf16 v[24:27], v[156:159], v[208:211], v[24:27]\n\t" \
"v_mfma_f32_16x16x32_bf16 v[32:35], v[160:163], v[208:211], v[32:35]\n\t" \
"s_nop 2\n\t" \
"v_rcp_f32 v236, v64\n\t" \
"v_mfma_f32_16x16x32_bf16 v[40:43], v[164:167], v[208:211], v[40:43]\n\t" \
"v_mfma_f32_16x16x32_bf16 v[48:51], v[168:171], v[208:211], v[48:51]\n\t" \
"v_mfma_f32_16x16x32_bf16 v[56:59], v[172:175], v[208:211], v[56:59]\n\t" \
"v_pk_mul_f32 v[0:1], v[0:1], v[236:237] op_sel_hi:[1,0]\n\t" \
"v_pk_mul_f32 v[2:3], v[2:3], v[236:237] op_sel_hi:[1,0]\n\t" \
"v_cvt_pk_bf16_f32 v192, v0, v1\n\t" \
"v_cvt_pk_bf16_f32 v193, v2, v3\n\t" \
"v_pk_mul_f32 v[8:9], v[8:9], v[236:237] op_sel_hi:[1,0]\n\t" \
"v_pk_mul_f32 v[10:11], v[10:11], v[236:237] op_sel_hi:[1,0]\n\t" \
"v_cvt_pk_bf16_f32 v194, v8, v9\n\t" \
"v_cvt_pk_bf16_f32 v195, v10, v11\n\t" \
"v_pk_mul_f32 v[16:17], v[16:17], v[236:237] op_sel_hi:[1,0]\n\t" \
"v_pk_mul_f32 v[18:19], v[18:19], v[236:237] op_sel_hi:[1,0]\n\t" \
"v_cvt_pk_bf16_f32 v196, v16, v17\n\t" \
"v_cvt_pk_bf16_f32 v197, v18, v19\n\t" \
"v_pk_mul_f32 v[24:25], v[24:25], v[236:237] op_sel_hi:[1,0]\n\t" \
"v_pk_mul_f32 v[26:27], v[26:27], v[236:237] op_sel_hi:[1,0]\n\t" \
"v_cvt_pk_bf16_f32 v198, v24, v25\n\t" \
"v_cvt_pk_bf16_f32 v199, v26, v27\n\t" \
"v_pk_mul_f32 v[32:33], v[32:33], v[236:237] op_sel_hi:[1,0]\n\t" \
"v_pk_mul_f32 v[34:35], v[34:35], v[236:237] op_sel_hi:[1,0]\n\t" \
"v_cvt_pk_bf16_f32 v200, v32, v33\n\t" \
"v_cvt_pk_bf16_f32 v201, v34, v35\n\t" \
"v_pk_mul_f32 v[40:41], v[40:41], v[236:237] op_sel_hi:[1,0]\n\t" \
"v_pk_mul_f32 v[42:43], v[42:43], v[236:237] op_sel_hi:[1,0]\n\t" \
"v_cvt_pk_bf16_f32 v202, v40, v41\n\t" \
"v_cvt_pk_bf16_f32 v203, v42, v43\n\t" \
"v_pk_mul_f32 v[48:49], v[48:49], v[236:237] op_sel_hi:[1,0]\n\t" \
"v_pk_mul_f32 v[50:51], v[50:51], v[236:237] op_sel_hi:[1,0]\n\t" \
"v_cvt_pk_bf16_f32 v204, v48, v49\n\t" \
"v_cvt_pk_bf16_f32 v205, v50, v51\n\t" \
"v_pk_mul_f32 v[56:57], v[56:57], v[236:237] op_sel_hi:[1,0]\n\t" \
"v_pk_mul_f32 v[58:59], v[58:59], v[236:237] op_sel_hi:[1,0]\n\t" \
"v_cvt_pk_bf16_f32 v206, v56, v57\n\t" \
"v_cvt_pk_bf16_f32 v207, v58, v59\n\t" \
"s_mov_b32 s68, 1\n\t" \
"s_branch L_item1_%=\n\t" \
"L_last1_%=:\n\t" \
"v_exp_f32 v232, v192\n\t" \
"v_exp_f32 v233, v193\n\t" \
"v_exp_f32 v234, v194\n\t" \
"v_cvt_pk_bf16_f32 v208, v232, v233\n\t" \
"v_exp_f32 v235, v195\n\t" \
"v_exp_f32 v232, v200\n\t" \
"v_cvt_pk_bf16_f32 v209, v234, v235\n\t" \
"v_exp_f32 v233, v201\n\t" \
"v_exp_f32 v234, v202\n\t" \
"v_cvt_pk_bf16_f32 v210, v232, v233\n\t" \
"v_exp_f32 v235, v203\n\t" \
"s_nop 0\n\t" \
"v_cvt_pk_bf16_f32 v211, v234, v235\n\t" \
"s_lshl_b32 s61, s32, 11\n\t" \
"v_add_u32 v240, s61, v250\n\t" \
"v_add_u32 v241, 0x10000, v240\n\t" \
"v_add_u32 v242, 0x20000, v240\n\t" \
"s_lshl_b32 s61, s32, 2\n\t" \
"s_add_u32 s61, s61, 0x7c10\n\t" \
"v_mov_b32 v243, s61\n\t" \
"v_mfma_f32_16x16x32_bf16 v[64:67], v[216:219], v[208:211], v[64:67]\n\t" \
"v_mfma_f32_16x16x32_bf16 v[0:3], v[144:147], v[208:211], v[0:3]\n\t" \
"v_mfma_f32_16x16x32_bf16 v[8:11], v[148:151], v[208:211], v[8:11]\n\t" \
"v_mfma_f32_16x16x32_bf16 v[16:19], v[152:155], v[208:211], v[16:19]\n\t" \
"v_mfma_f32_16x16x32_bf16 v[24:27], v[156:159], v[208:211], v[24:27]\n\t" \
"v_mfma_f32_16x16x32_bf16 v[32:35], v[160:163], v[208:211], v[32:35]\n\t" \
"s_nop 2\n\t" \
"v_rcp_f32 v236, v64\n\t" \
"v_mfma_f32_16x16x32_bf16 v[40:43], v[164:167], v[208:211], v[40:43]\n\t" \
"v_mfma_f32_16x16x32_bf16 v[48:51], v[168:171], v[208:211], v[48:51]\n\t" \
"v_mfma_f32_16x16x32_bf16 v[56:59], v[172:175], v[208:211], v[56:59]\n\t" \
"v_pk_mul_f32 v[0:1], v[0:1], v[236:237] op_sel_hi:[1,0]\n\t" \
"v_pk_mul_f32 v[2:3], v[2:3], v[236:237] op_sel_hi:[1,0]\n\t" \
"v_cvt_pk_bf16_f32 v192, v0, v1\n\t" \
"v_cvt_pk_bf16_f32 v193, v2, v3\n\t" \
"v_pk_mul_f32 v[8:9], v[8:9], v[236:237] op_sel_hi:[1,0]\n\t" \
"v_pk_mul_f32 v[10:11], v[10:11], v[236:237] op_sel_hi:[1,0]\n\t" \
"v_cvt_pk_bf16_f32 v194, v8, v9\n\t" \
"v_cvt_pk_bf16_f32 v195, v10, v11\n\t" \
"ds_write_b128 v242, v[192:195] offset:0\n\t" \
"v_pk_mul_f32 v[16:17], v[16:17], v[236:237] op_sel_hi:[1,0]\n\t" \
"v_pk_mul_f32 v[18:19], v[18:19], v[236:237] op_sel_hi:[1,0]\n\t" \
"v_cvt_pk_bf16_f32 v196, v16, v17\n\t" \
"v_cvt_pk_bf16_f32 v197, v18, v19\n\t" \
"v_pk_mul_f32 v[24:25], v[24:25], v[236:237] op_sel_hi:[1,0]\n\t" \
"v_pk_mul_f32 v[26:27], v[26:27], v[236:237] op_sel_hi:[1,0]\n\t" \
"v_cvt_pk_bf16_f32 v198, v24, v25\n\t" \
"v_cvt_pk_bf16_f32 v199, v26, v27\n\t" \
"ds_write_b128 v242, v[196:199] offset:8192\n\t" \
"v_mov_b32 v232, 1\n\t" \
"s_waitcnt lgkmcnt(0)\n\t" \
"ds_write_b32 v243, v232\n\t" \
"v_pk_mul_f32 v[32:33], v[32:33], v[236:237] op_sel_hi:[1,0]\n\t" \
"v_pk_mul_f32 v[34:35], v[34:35], v[236:237] op_sel_hi:[1,0]\n\t" \
"v_cvt_pk_bf16_f32 v200, v32, v33\n\t" \
"v_cvt_pk_bf16_f32 v201, v34, v35\n\t" \
"v_pk_mul_f32 v[40:41], v[40:41], v[236:237] op_sel_hi:[1,0]\n\t" \
"v_pk_mul_f32 v[42:43], v[42:43], v[236:237] op_sel_hi:[1,0]\n\t" \
"v_cvt_pk_bf16_f32 v202, v40, v41\n\t" \
"v_cvt_pk_bf16_f32 v203, v42, v43\n\t" \
"ds_write_b128 v241, v[200:203] offset:32768\n\t" \
"v_pk_mul_f32 v[48:49], v[48:49], v[236:237] op_sel_hi:[1,0]\n\t" \
"v_pk_mul_f32 v[50:51], v[50:51], v[236:237] op_sel_hi:[1,0]\n\t" \
"v_cvt_pk_bf16_f32 v204, v48, v49\n\t" \
"v_cvt_pk_bf16_f32 v205, v50, v51\n\t" \
"v_pk_mul_f32 v[56:57], v[56:57], v[236:237] op_sel_hi:[1,0]\n\t" \
"v_pk_mul_f32 v[58:59], v[58:59], v[236:237] op_sel_hi:[1,0]\n\t" \
"v_cvt_pk_bf16_f32 v206, v56, v57\n\t" \
"v_cvt_pk_bf16_f32 v207, v58, v59\n\t" \
"ds_write_b128 v241, v[204:207] offset:40960\n\t" \
"v_mov_b32 v232, 2\n\t" \
"s_waitcnt lgkmcnt(0)\n\t" \
"ds_write_b32 v243, v232\n\t" \
"s_branch L_atail_%=\n\t" \
"L_rare6_%=:\n\t" \
"s_nop 7\n\t" \
"v_maximum3_f32 v236, v192, v193, v194\n\t" \
"v_maximum3_f32 v236, v236, v195, v200\n\t" \
"v_maximum3_f32 v236, v236, v201, v202\n\t" \
"v_maximum3_f32 v236, v236, v203, v203\n\t" \
"v_mov_b32 v237, v236\n\t" \
"s_nop 1\n\t" \
"v_permlane16_swap_b32 v236, v237\n\t" \
"v_maximum3_f32 v236, v236, v237, v237\n\t" \
"v_mov_b32 v237, v236\n\t" \
"s_nop 1\n\t" \
"v_permlane32_swap_b32 v236, v237\n\t" \
"v_maximum3_f32 v236, v236, v237, 0\n\t" \
"v_sub_f32 v192, v192, v236\n\t" \
"v_sub_f32 v193, v193, v236\n\t" \
"v_sub_f32 v194, v194, v236\n\t" \
"v_sub_f32 v195, v195, v236\n\t" \
"v_sub_f32 v200, v200, v236\n\t" \
"v_sub_f32 v201, v201, v236\n\t" \
"v_sub_f32 v202, v202, v236\n\t" \
"v_sub_f32 v203, v203, v236\n\t" \
"v_sub_f32 v72, v72, v236\n\t" \
"v_sub_f32 v73, v73, v236\n\t" \
"v_sub_f32 v74, v74, v236\n\t" \
"v_sub_f32 v75, v75, v236\n\t" \
"v_exp_f32 v236, -v236\n\t" \
"s_nop 0\n\t" \
"v_mul_f32 v64, v64, v236\n\t" \
"v_mul_f32 v65, v65, v236\n\t" \
"v_mul_f32 v66, v66, v236\n\t" \
"v_mul_f32 v67, v67, v236\n\t" \
"v_mul_f32 v0, v0, v236\n\t" \
"v_mul_f32 v1, v1, v236\n\t" \
"v_mul_f32 v2, v2, v236\n\t" \
"v_mul_f32 v3, v3, v236\n\t" \
"v_mul_f32 v8, v8, v236\n\t" \
"v_mul_f32 v9, v9, v236\n\t" \
"v_mul_f32 v10, v10, v236\n\t" \
"v_mul_f32 v11, v11, v236\n\t" \
"v_mul_f32 v16, v16, v236\n\t" \
"v_mul_f32 v17, v17, v236\n\t" \
"v_mul_f32 v18, v18, v236\n\t" \
"v_mul_f32 v19, v19, v236\n\t" \
"v_mul_f32 v24, v24, v236\n\t" \
"v_mul_f32 v25, v25, v236\n\t" \
"v_mul_f32 v26, v26, v236\n\t" \
"v_mul_f32 v27, v27, v236\n\t" \
"v_mul_f32 v32, v32, v236\n\t" \
"v_mul_f32 v33, v33, v236\n\t" \
"v_mul_f32 v34, v34, v236\n\t" \
"v_mul_f32 v35, v35, v236\n\t" \
"v_mul_f32 v40, v40, v236\n\t" \
"v_mul_f32 v41, v41, v236\n\t" \
"v_mul_f32 v42, v42, v236\n\t" \
"v_mul_f32 v43, v43, v236\n\t" \
"v_mul_f32 v48, v48, v236\n\t" \
"v_mul_f32 v49, v49, v236\n\t" \
"v_mul_f32 v50, v50, v236\n\t" \
"v_mul_f32 v51, v51, v236\n\t" \
"v_mul_f32 v56, v56, v236\n\t" \
"v_mul_f32 v57, v57, v236\n\t" \
"v_mul_f32 v58, v58, v236\n\t" \
"v_mul_f32 v59, v59, v236\n\t" \
"s_nop 1\n\t" \
"s_branch L_back6_%=\n\t" \
"L_rare7_%=:\n\t" \
"s_nop 7\n\t" \
"v_maximum3_f32 v236, v176, v177, v178\n\t" \
"v_maximum3_f32 v236, v236, v179, v184\n\t" \
"v_maximum3_f32 v236, v236, v185, v186\n\t" \
"v_maximum3_f32 v236, v236, v187, v187\n\t" \
"v_mov_b32 v237, v236\n\t" \
"s_nop 1\n\t" \
"v_permlane16_swap_b32 v236, v237\n\t" \
"v_maximum3_f32 v236, v236, v237, v237\n\t" \
"v_mov_b32 v237, v236\n\t" \
"s_nop 1\n\t" \
"v_permlane32_swap_b32 v236, v237\n\t" \
"v_maximum3_f32 v236, v236, v237, 0\n\t" \
"v_sub_f32 v176, v176, v236\n\t" \
"v_sub_f32 v177, v177, v236\n\t" \
"v_sub_f32 v178, v178, v236\n\t" \
"v_sub_f32 v179, v179, v236\n\t" \
"v_sub_f32 v184, v184, v236\n\t" \
"v_sub_f32 v185, v185, v236\n\t" \
"v_sub_f32 v186, v186, v236\n\t" \
"v_sub_f32 v187, v187, v236\n\t" \
"v_sub_f32 v72, v72, v236\n\t" \
"v_sub_f32 v73, v73, v236\n\t" \
"v_sub_f32 v74, v74, v236\n\t" \
"v_sub_f32 v75, v75, v236\n\t" \
"v_exp_f32 v236, -v236\n\t" \
"s_nop 0\n\t" \
"v_mul_f32 v64, v64, v236\n\t" \
"v_mul_f32 v65, v65, v236\n\t" \
"v_mul_f32 v66, v66, v236\n\t" \
"v_mul_f32 v67, v67, v236\n\t" \
"v_mul_f32 v0, v0, v236\n\t" \
"v_mul_f32 v1, v1, v236\n\t" \
"v_mul_f32 v2, v2, v236\n\t" \
"v_mul_f32 v3, v3, v236\n\t" \
"v_mul_f32 v8, v8, v236\n\t" \
"v_mul_f32 v9, v9, v236\n\t" \
"v_mul_f32 v10, v10, v236\n\t" \
"v_mul_f32 v11, v11, v236\n\t" \
"v_mul_f32 v16, v16, v236\n\t" \
"v_mul_f32 v17, v17, v236\n\t" \
"v_mul_f32 v18, v18, v236\n\t" \
"v_mul_f32 v19, v19, v236\n\t" \
"v_mul_f32 v24, v24, v236\n\t" \
"v_mul_f32 v25, v25, v236\n\t" \
"v_mul_f32 v26, v26, v236\n\t" \
"v_mul_f32 v27, v27, v236\n\t" \
"v_mul_f32 v32, v32, v236\n\t" \
"v_mul_f32 v33, v33, v236\n\t" \
"v_mul_f32 v34, v34, v236\n\t" \
"v_mul_f32 v35, v35, v236\n\t" \
"v_mul_f32 v40, v40, v236\n\t" \
"v_mul_f32 v41, v41, v236\n\t" \
"v_mul_f32 v42, v42, v236\n\t" \
"v_mul_f32 v43, v43, v236\n\t" \
"v_mul_f32 v48, v48, v236\n\t" \
"v_mul_f32 v49, v49, v236\n\t" \
"v_mul_f32 v50, v50, v236\n\t" \
"v_mul_f32 v51, v51, v236\n\t" \
"v_mul_f32 v56, v56, v236\n\t" \
"v_mul_f32 v57, v57, v236\n\t" \
"v_mul_f32 v58, v58, v236\n\t" \
"v_mul_f32 v59, v59, v236\n\t" \
"s_nop 1\n\t" \
"s_branch L_back7_%=\n\t" \
"L_rare8_%=:\n\t" \
"s_nop 7\n\t" \
"v_maximum3_f32 v236, v192, v193, v194\n\t" \
"v_maximum3_f32 v236, v236, v195, v200\n\t" \
"v_maximum3_f32 v236, v236, v201, v202\n\t" \
"v_maximum3_f32 v236, v236, v203, v203\n\t" \
"v_mov_b32 v237, v236\n\t" \
"s_nop 1\n\t" \
"v_permlane16_swap_b32 v236, v237\n\t" \
"v_maximum3_f32 v236, v236, v237, v237\n\t" \
"v_mov_b32 v237, v236\n\t" \
"s_nop 1\n\t" \
"v_permlane32_swap_b32 v236, v237\n\t" \
"v_maximum3_f32 v236, v236, v237, 0\n\t" \
"v_sub_f32 v192, v192, v236\n\t" \
"v_sub_f32 v193, v193, v236\n\t" \
"v_sub_f32 v194, v194, v236\n\t" \
"v_sub_f32 v195, v195, v236\n\t" \
"v_sub_f32 v200, v200, v236\n\t" \
"v_sub_f32 v201, v201, v236\n\t" \
"v_sub_f32 v202, v202, v236\n\t" \
"v_sub_f32 v203, v203, v236\n\t" \
"v_sub_f32 v72, v72, v236\n\t" \
"v_sub_f32 v73, v73, v236\n\t" \
"v_sub_f32 v74, v74, v236\n\t" \
"v_sub_f32 v75, v75, v236\n\t" \
"v_exp_f32 v236, -v236\n\t" \
"s_nop 0\n\t" \
"v_mul_f32 v64, v64, v236\n\t" \
"v_mul_f32 v65, v65, v236\n\t" \
"v_mul_f32 v66, v66, v236\n\t" \
"v_mul_f32 v67, v67, v236\n\t" \
"v_mul_f32 v0, v0, v236\n\t" \
"v_mul_f32 v1, v1, v236\n\t" \
"v_mul_f32 v2, v2, v236\n\t" \
"v_mul_f32 v3, v3, v236\n\t" \
"v_mul_f32 v8, v8, v236\n\t" \
"v_mul_f32 v9, v9, v236\n\t" \
"v_mul_f32 v10, v10, v236\n\t" \
"v_mul_f32 v11, v11, v236\n\t" \
"v_mul_f32 v16, v16, v236\n\t" \
"v_mul_f32 v17, v17, v236\n\t" \
"v_mul_f32 v18, v18, v236\n\t" \
"v_mul_f32 v19, v19, v236\n\t" \
"v_mul_f32 v24, v24, v236\n\t" \
"v_mul_f32 v25, v25, v236\n\t" \
"v_mul_f32 v26, v26, v236\n\t" \
"v_mul_f32 v27, v27, v236\n\t" \
"v_mul_f32 v32, v32, v236\n\t" \
"v_mul_f32 v33, v33, v236\n\t" \
"v_mul_f32 v34, v34, v236\n\t" \
"v_mul_f32 v35, v35, v236\n\t" \
"v_mul_f32 v40, v40, v236\n\t" \
"v_mul_f32 v41, v41, v236\n\t" \
"v_mul_f32 v42, v42, v236\n\t" \
"v_mul_f32 v43, v43, v236\n\t" \
"v_mul_f32 v48, v48, v236\n\t" \
"v_mul_f32 v49, v49, v236\n\t" \
"v_mul_f32 v50, v50, v236\n\t" \
"v_mul_f32 v51, v51, v236\n\t" \
"v_mul_f32 v56, v56, v236\n\t" \
"v_mul_f32 v57, v57, v236\n\t" \
"v_mul_f32 v58, v58, v236\n\t" \
"v_mul_f32 v59, v59, v236\n\t" \
"s_nop 1\n\t" \
"s_branch L_back8_%=\n\t" \
"L_atail_%=:\n\t" \
"s_setprio 0\n\t" \
"v_mov_b32 v20, 0x7c00\n\t" \
"s_lshl_b32 s64, s32, 3\n\t" \
"s_add_u32 s64, s64, s83\n\t" \
"s_cmp_ge_u32 s32, 2\n\t" \
"s_cbranch_scc1 L_atail2_%=\n\t" \
"v_mbcnt_lo_u32_b32 v0, -1, 0\n\t" \
"v_mbcnt_hi_u32_b32 v0, -1, v0\n\t" \
"s_mov_b32 s71, 0\n\t" \
"s_mov_b32 s78, 0\n\t" \
"v_lshrrev_b32 v5, 5, v0\n\t" \
"v_and_b32 v6, 31, v0\n\t" \
"v_lshlrev_b32 v7, 11, v5\n\t" \
"s_lshl_b32 s60, s71, 15\n\t" \
"v_add_u32 v7, s60, v7\n\t" \
"v_lshlrev_b32 v5, 1, v5\n\t" \
"v_xor_b32 v6, v6, v5\n\t" \
"v_xor_b32 v5, 0, v6\n\t" \
"v_lshl_add_u32 v5, v5, 4, v7\n\t" \
"v_add_u32 v10, 0, v5\n\t" \
"v_xor_b32 v5, 4, v6\n\t" \
"v_lshl_add_u32 v5, v5, 4, v7\n\t" \
"v_add_u32 v11, 3072, v5\n\t" \
"v_xor_b32 v5, 8, v6\n\t" \
"v_lshl_add_u32 v5, v5, 4, v7\n\t" \
"v_add_u32 v12, 6144, v5\n\t" \
"v_xor_b32 v5, 12, v6\n\t" \
"v_lshl_add_u32 v5, v5, 4, v7\n\t" \
"v_add_u32 v13, 9216, v5\n\t" \
"s_lshl_b32 s60, s32, 2\n\t" \
"s_add_u32 s60, s60, 0x7c00\n\t" \
"v_mov_b32 v21, s60\n\t" \
"s_add_u32 s70, 8, s32\n\t" \
"s_cmp_eq_u32 s32, 0\n\t" \
"s_mov_b32 s65, 0x27359\n\t" \
"s_cselect_b32 s65, 0x59273, s65\n\t" \
"s_mov_b32 s75, 1\n\t" \
"L_wl_%=:\n\t" \
"s_sub_u32 s68, s70, 4\n\t" \
"s_and_b32 s67, s65, 15\n\t" \
"s_lshr_b32 s65, s65, 4\n\t" \
"s_lshl_b32 s67, s67, 16\n\t" \
"s_or_b32 s65, s65, s67\n\t" \
"s_lshr_b32 s67, s67, 2\n\t" \
"s_and_b32 s60, s70, 28\n\t" \
"s_lshr_b32 s60, 0xba932180, s60\n\t" \
"s_and_b32 s62, s60, 8\n\t" \
"s_cselect_b32 s62, 4, 0\n\t" \
"s_add_u32 s60, s60, s37\n\t" \
"s_and_b32 s60, s60, 3\n\t" \
"s_sub_u32 s60, s60, s37\n\t" \
"s_add_u32 s60, s60, s39\n\t" \
"s_add_u32 s60, s60, s62\n\t" \
"s_and_b32 s63, s70, 3\n\t" \
"s_mul_i32 s61, s60, 28672\n\t" \
"s_add_u32 s61, s61, s82\n\t" \
"s_lshl_b32 s62, s60, 18\n\t" \
"s_mul_i32 s60, s63, 7168\n\t" \
"s_add_u32 s61, s61, s60\n\t" \
"s_lshl_b32 s63, s63, 16\n\t" \
"s_add_u32 s62, s62, s63\n\t" \
"s_mov_b32 s3, 200000\n\t" \
"L_sl9_%=:\n\t" \
"ds_read_b128 v[24:27], v20 offset:32\n\t" \
"s_sub_u32 s3, s3, 1\n\t" \
"s_cbranch_scc1 L_sd9_%=\n\t" \
"s_waitcnt lgkmcnt(0)\n\t" \
"v_min3_u32 v24, v24, v25, v26\n\t" \
"v_min_u32 v24, v24, v27\n\t" \
"v_cmp_gt_u32 vcc, s68, v24\n\t" \
"s_cbranch_vccnz L_sl9_%=\n\t" \
"L_sd9_%=:\n\t" \
"s_waitcnt lgkmcnt(0)\n\t" \
"s_add_u32 m0, s78, s67\n\t" \
"s_nop 0\n\t" \
"buffer_load_dwordx4 v10, s[20:23], s62 offen offset:0 lds\n\t" \
"buffer_load_dwordx4 v11, s[20:23], s62 offen offset:1024 lds\n\t" \
"buffer_load_dwordx4 v12, s[20:23], s62 offen offset:2048 lds\n\t" \
"buffer_load_dwordx4 v13, s[20:23], s62 offen offset:3072 lds\n\t" \
"s_add_u32 m0, m0, 4096\n\t" \
"s_add_u32 s62, s62, 0x4000\n\t" \
"buffer_load_dwordx4 v10, s[20:23], s62 offen offset:0 lds\n\t" \
"buffer_load_dwordx4 v11, s[20:23], s62 offen offset:1024 lds\n\t" \
"buffer_load_dwordx4 v12, s[20:23], s62 offen offset:2048 lds\n\t" \
"buffer_load_dwordx4 v13, s[20:23], s62 offen offset:3072 lds\n\t" \
"s_add_u32 s67, s67, 0x2000\n\t" \
"s_add_u32 s62, s62, 0x4000\n\t" \
"s_add_u32 m0, s78, s67\n\t" \
"s_nop 0\n\t" \
"buffer_load_dwordx4 v10, s[20:23], s62 offen offset:0 lds\n\t" \
"buffer_load_dwordx4 v11, s[20:23], s62 offen offset:1024 lds\n\t" \
"buffer_load_dwordx4 v12, s[20:23], s62 offen offset:2048 lds\n\t" \
"buffer_load_dwordx4 v13, s[20:23], s62 offen offset:3072 lds\n\t" \
"s_add_u32 m0, m0, 4096\n\t" \
"s_add_u32 s62, s62, 0x4000\n\t" \
"buffer_load_dwordx4 v10, s[20:23], s62 offen offset:0 lds\n\t" \
"buffer_load_dwordx4 v11, s[20:23], s62 offen offset:1024 lds\n\t" \
"buffer_load_dwordx4 v12, s[20:23], s62 offen offset:2048 lds\n\t" \
"buffer_load_dwordx4 v13, s[20:23], s62 offen offset:3072 lds\n\t" \
"s_cmp_lt_u32 s70, 10\n\t" \
"s_cbranch_scc1 L_wl2_%=\n\t" \
"s_sub_u32 s68, s70, 1\n\t" \
"s_waitcnt vmcnt(16)\n\t" \
"v_mov_b32 v22, s68\n\t" \
"ds_write_b32 v21, v22\n\t" \
"L_wl2_%=:\n\t" \
"s_lshr_b32 s60, s70, 1\n\t" \
"s_cmp_eq_u32 s60, 6\n\t" \
"s_cbranch_scc0 L_wh10_%=\n\t" \
"s_lshl_b32 s60, s32, 11\n\t" \
"s_add_u32 s60, s60, s56\n\t" \
"s_add_u32 s60, s60, 114688\n\t" \
"s_cmp_eq_u32 s32, 3\n\t" \
"s_cbranch_scc1 L_os111_%=\n\t" \
"s_add_u32 s63, s60, 0\n\t" \
"buffer_store_dwordx4 v[144:147], v250, s[24:27], s63 offen offset:1024 sc1\n\t" \
"s_add_u32 s63, s60, 7168\n\t" \
"buffer_store_dwordx4 v[148:151], v250, s[24:27], s63 offen offset:1024 sc1\n\t" \
"s_add_u32 s63, s60, 14336\n\t" \
"buffer_store_dwordx4 v[152:155], v250, s[24:27], s63 offen offset:1024 sc1\n\t" \
"s_add_u32 s63, s60, 21504\n\t" \
"buffer_store_dwordx4 v[156:159], v250, s[24:27], s63 offen offset:1024 sc1\n\t" \
"L_os111_%=:\n\t" \
"s_add_u32 s63, s60, 0\n\t" \
"buffer_store_dwordx4 v[192:195], v250, s[24:27], s63 offen offset:0 sc1\n\t" \
"s_add_u32 s63, s60, 7168\n\t" \
"buffer_store_dwordx4 v[196:199], v250, s[24:27], s63 offen offset:0 sc1\n\t" \
"s_add_u32 s63, s60, 14336\n\t" \
"buffer_store_dwordx4 v[200:203], v250, s[24:27], s63 offen offset:0 sc1\n\t" \
"s_add_u32 s63, s60, 21504\n\t" \
"buffer_store_dwordx4 v[204:207], v250, s[24:27], s63 offen offset:0 sc1\n\t" \
"L_wh10_%=:\n\t" \
"s_lshr_b32 s60, s70, 1\n\t" \
"s_cmp_eq_u32 s60, 7\n\t" \
"s_cbranch_scc0 L_wh12_%=\n\t" \
"v_mov_b32 v28, s50\n\t" \
"v_mov_b32 v29, s51\n\t" \
"s_mov_b64 exec, 1\n\t" \
"buffer_store_dwordx2 v[28:29], off, s[24:27], s64 offset:96 sc1\n\t" \
"s_mov_b64 exec, -1\n\t" \
"L_wh12_%=:\n\t" \
"s_add_u32 s70, s70, 2\n\t" \
"s_cmp_lt_u32 s70, 32\n\t" \
"s_cbranch_scc1 L_wl_%=\n\t" \
"s_sub_u32 s68, s70, 1\n\t" \
"s_waitcnt vmcnt(0)\n\t" \
"v_mov_b32 v22, s68\n\t" \
"ds_write_b32 v21, v22\n\t" \
"s_branch L_end_%=\n\t" \
"L_atail2_%=:\n\t" \
"v_mbcnt_lo_u32_b32 v0, -1, 0\n\t" \
"v_mbcnt_hi_u32_b32 v0, -1, v0\n\t" \
"v_and_b32 v5, 3, v0\n\t" \
"v_lshl_add_u32 v10, v5, 3, v254\n\t" \
"v_add_u32 v11, 96, v10\n\t" \
"v_add_u32 v10, 64, v10\n\t" \
"v_lshlrev_b32 v8, 4, v0\n\t" \
"s_sub_u32 s70, 11, s32\n\t" \
"s_and_b32 s60, s70, 1\n\t" \
"s_lshl_b32 s75, s60, 13\n\t" \
"s_lshl_b32 s60, s60, 2\n\t" \
"s_add_u32 s60, s60, 0x7c08\n\t" \
"v_mov_b32 v21, s60\n\t" \
"s_mov_b32 s65, 0x68104\n\t" \
"s_mov_b64 exec, 0x3333\n\t" \
"s_mov_b32 s3, 200000\n\t" \
"L_pl13_%=:\n\t" \
"buffer_load_dwordx2 v[30:31], v10, s[24:27], 0 offen sc1\n\t" \
"s_sub_u32 s3, s3, 1\n\t" \
"s_cbranch_scc1 L_pd13_%=\n\t" \
"s_waitcnt vmcnt(0)\n\t" \
"L_pc13_%=:\n\t" \
"v_cmp_ne_u64 vcc, v[30:31], v[252:253]\n\t" \
"s_cbranch_vccnz L_pl13_%=\n\t" \
"L_pd13_%=:\n\t" \
"s_waitcnt vmcnt(0)\n\t" \
"s_mov_b64 exec, -1\n\t" \
"L_ol_%=:\n\t" \
"s_lshr_b32 s60, s70, 1\n\t" \
"s_cmp_eq_u32 s60, 10\n\t" \
"s_cbranch_scc0 L_wh14_%=\n\t" \
"s_sub_u32 s68, s70, 1\n\t" \
"s_waitcnt vmcnt(0)\n\t" \
"v_mov_b32 v22, s68\n\t" \
"ds_write_b32 v21, v22\n\t" \
"s_mov_b64 exec, 0xffff\n\t" \
"s_mov_b32 s3, 200000\n\t" \
"L_pl15_%=:\n\t" \
"buffer_load_dwordx2 v[30:31], v11, s[24:27], 0 offen sc1\n\t" \
"s_sub_u32 s3, s3, 1\n\t" \
"s_cbranch_scc1 L_pd15_%=\n\t" \
"s_waitcnt vmcnt(0)\n\t" \
"L_pc15_%=:\n\t" \
"v_cmp_ne_u64 vcc, v[30:31], v[252:253]\n\t" \
"s_cbranch_vccnz L_pl15_%=\n\t" \
"L_pd15_%=:\n\t" \
"s_waitcnt vmcnt(0)\n\t" \
"s_mov_b64 exec, -1\n\t" \
"L_wh14_%=:\n\t" \
"s_and_b32 s67, s65, 15\n\t" \
"s_lshr_b32 s65, s65, 4\n\t" \
"s_lshl_b32 s67, s67, 16\n\t" \
"s_or_b32 s65, s65, s67\n\t" \
"s_lshr_b32 s67, s67, 2\n\t" \
"s_and_b32 s60, s70, 28\n\t" \
"s_lshr_b32 s60, 0xba932180, s60\n\t" \
"s_and_b32 s62, s60, 8\n\t" \
"s_cselect_b32 s62, 4, 0\n\t" \
"s_add_u32 s60, s60, s37\n\t" \
"s_and_b32 s60, s60, 3\n\t" \
"s_sub_u32 s60, s60, s37\n\t" \
"s_add_u32 s60, s60, s39\n\t" \
"s_add_u32 s60, s60, s62\n\t" \
"s_and_b32 s63, s70, 3\n\t" \
"s_mul_i32 s61, s60, 28672\n\t" \
"s_add_u32 s61, s61, s82\n\t" \
"s_lshl_b32 s62, s60, 18\n\t" \
"s_mul_i32 s60, s63, 7168\n\t" \
"s_add_u32 s61, s61, s60\n\t" \
"s_lshl_b32 s63, s63, 16\n\t" \
"s_add_u32 s62, s62, s63\n\t" \
"s_cmp_lt_u32 s70, 12\n\t" \
"s_cbranch_scc1 L_ol2_%=\n\t" \
"s_sub_u32 s68, s70, 9\n\t" \
"s_mov_b32 s3, 200000\n\t" \
"L_sl16_%=:\n\t" \
"ds_read_b128 v[24:27], v20 offset:32\n\t" \
"s_sub_u32 s3, s3, 1\n\t" \
"s_cbranch_scc1 L_sd16_%=\n\t" \
"s_waitcnt lgkmcnt(0)\n\t" \
"v_min3_u32 v24, v24, v25, v26\n\t" \
"v_min_u32 v24, v24, v27\n\t" \
"v_cmp_gt_u32 vcc, s68, v24\n\t" \
"s_cbranch_vccnz L_sl16_%=\n\t" \
"L_sd16_%=:\n\t" \
"s_waitcnt lgkmcnt(0)\n\t" \
"L_ol2_%=:\n\t" \
"s_add_u32 m0, s67, s75\n\t" \
"s_nop 0\n\t" \
"buffer_load_dwordx4 v8, s[24:27], s61 offen offset:0 sc1 lds\n\t" \
"buffer_load_dwordx4 v8, s[24:27], s61 offen offset:1024 sc1 lds\n\t" \
"buffer_load_dwordx4 v8, s[24:27], s61 offen offset:2048 sc1 lds\n\t" \
"buffer_load_dwordx4 v8, s[24:27], s61 offen offset:3072 sc1 lds\n\t" \
"s_add_u32 m0, m0, 4096\n\t" \
"s_add_u32 s61, s61, 4096\n\t" \
"buffer_load_dwordx4 v8, s[24:27], s61 offen offset:0 sc1 lds\n\t" \
"buffer_load_dwordx4 v8, s[24:27], s61 offen offset:1024 sc1 lds\n\t" \
"buffer_load_dwordx4 v8, s[24:27], s61 offen offset:2048 sc1 lds\n\t" \
"s_cmp_lt_u32 s70, 10\n\t" \
"s_cbranch_scc1 L_ol4_%=\n\t" \
"s_sub_u32 s68, s70, 1\n\t" \
"s_waitcnt vmcnt(7)\n\t" \
"v_mov_b32 v22, s68\n\t" \
"ds_write_b32 v21, v22\n\t" \
"L_ol4_%=:\n\t" \
"s_lshr_b32 s60, s70, 1\n\t" \
"s_cmp_eq_u32 s60, 8\n\t" \
"s_cbranch_scc0 L_wh17_%=\n\t" \
"s_lshl_b32 s60, s32, 11\n\t" \
"s_add_u32 s60, s60, s56\n\t" \
"s_add_u32 s60, s60, 114688\n\t" \
"s_cmp_eq_u32 s32, 3\n\t" \
"s_cbranch_scc1 L_os118_%=\n\t" \
"s_add_u32 s63, s60, 0\n\t" \
"buffer_store_dwordx4 v[144:147], v250, s[24:27], s63 offen offset:1024 sc1\n\t" \
"s_add_u32 s63, s60, 7168\n\t" \
"buffer_store_dwordx4 v[148:151], v250, s[24:27], s63 offen offset:1024 sc1\n\t" \
"s_add_u32 s63, s60, 14336\n\t" \
"buffer_store_dwordx4 v[152:155], v250, s[24:27], s63 offen offset:1024 sc1\n\t" \
"s_add_u32 s63, s60, 21504\n\t" \
"buffer_store_dwordx4 v[156:159], v250, s[24:27], s63 offen offset:1024 sc1\n\t" \
"L_os118_%=:\n\t" \
"s_add_u32 s63, s60, 0\n\t" \
"buffer_store_dwordx4 v[192:195], v250, s[24:27], s63 offen offset:0 sc1\n\t" \
"s_add_u32 s63, s60, 7168\n\t" \
"buffer_store_dwordx4 v[196:199], v250, s[24:27], s63 offen offset:0 sc1\n\t" \
"s_add_u32 s63, s60, 14336\n\t" \
"buffer_store_dwordx4 v[200:203], v250, s[24:27], s63 offen offset:0 sc1\n\t" \
"s_add_u32 s63, s60, 21504\n\t" \
"buffer_store_dwordx4 v[204:207], v250, s[24:27], s63 offen offset:0 sc1\n\t" \
"L_wh17_%=:\n\t" \
"s_lshr_b32 s60, s70, 1\n\t" \
"s_cmp_eq_u32 s60, 9\n\t" \
"s_cbranch_scc0 L_wh19_%=\n\t" \
"v_mov_b32 v28, s50\n\t" \
"v_mov_b32 v29, s51\n\t" \
"s_mov_b64 exec, 1\n\t" \
"buffer_store_dwordx2 v[28:29], off, s[24:27], s64 offset:96 sc1\n\t" \
"s_mov_b64 exec, -1\n\t" \
"L_wh19_%=:\n\t" \
"s_lshr_b32 s60, s70, 1\n\t" \
"s_cmp_eq_u32 s60, 8\n\t" \
"s_cbranch_scc0 L_wh20_%=\n\t" \
"s_cmp_eq_u32 s32, 2\n\t" \
"s_cbranch_scc0 L_ntb_%=\n\t" \
"v_lshrrev_b32 v5, 4, v0\n\t" \
"v_bfe_u32 v6, v0, 2, 2\n\t" \
"v_lshl_add_u32 v5, v5, 5, v6\n\t" \
"v_lshlrev_b32 v5, 11, v5\n\t" \
"v_and_b32 v6, 3, v0\n\t" \
"v_lshl_add_u32 v5, v6, 7, v5\n\t" \
"s_lshl_b32 s60, s35, 13\n\t" \
"v_add_u32 v12, s60, v5\n\t" \
"s_add_u32 s60, s37, 1\n\t" \
"s_and_b32 s60, s60, 3\n\t" \
"s_sub_u32 s60, s60, s37\n\t" \
"s_add_u32 s60, s60, s39\n\t" \
"s_add_u32 s60, s60, 4\n\t" \
"s_mul_i32 s61, s60, 28672\n\t" \
"s_add_u32 s61, s61, s82\n\t" \
"s_lshl_b32 s62, s60, 18\n\t" \
"buffer_load_dword v9, v12, s[20:23], s62 offen\n\t" \
"s_add_u32 s60, s37, 2\n\t" \
"s_and_b32 s60, s60, 3\n\t" \
"s_sub_u32 s60, s60, s37\n\t" \
"s_add_u32 s60, s60, s39\n\t" \
"s_add_u32 s60, s60, 4\n\t" \
"s_mul_i32 s61, s60, 28672\n\t" \
"s_add_u32 s61, s61, s82\n\t" \
"s_lshl_b32 s62, s60, 18\n\t" \
"buffer_load_dword v9, v12, s[20:23], s62 offen\n\t" \
"s_add_u32 s60, s37, 3\n\t" \
"s_and_b32 s60, s60, 3\n\t" \
"s_sub_u32 s60, s60, s37\n\t" \
"s_add_u32 s60, s60, s39\n\t" \
"s_add_u32 s60, s60, 4\n\t" \
"s_mul_i32 s61, s60, 28672\n\t" \
"s_add_u32 s61, s61, s82\n\t" \
"s_lshl_b32 s62, s60, 18\n\t" \
"buffer_load_dword v9, v12, s[20:23], s62 offen\n\t" \
"L_ntb_%=:\n\t" \
"L_wh20_%=:\n\t" \
"s_add_u32 s70, s70, 2\n\t" \
"s_cmp_lt_u32 s70, 32\n\t" \
"s_cbranch_scc1 L_ol_%=\n\t" \
"s_sub_u32 s68, s70, 1\n\t" \
"s_waitcnt vmcnt(0)\n\t" \
"v_mov_b32 v22, s68\n\t" \
"ds_write_b32 v21, v22\n\t" \
"s_branch L_end_%=\n\t" \
"L_gemm_%=:\n\t" \
"s_and_b32 s32, s32, 3\n\t" \
"s_lshr_b32 s74, s32, 1\n\t" \
"v_and_b32 v5, 1, v2\n\t" \
"v_lshl_or_b32 v5, v5, 2, v3\n\t" \
"v_lshlrev_b32 v5, 1, v5\n\t" \
"v_lshrrev_b32 v6, 1, v4\n\t" \
"s_lshl_b32 s60, s32, 2\n\t" \
"v_or_b32 v6, s60, v6\n\t" \
"v_xor_b32 v5, v5, v6\n\t" \
"v_lshlrev_b32 v5, 4, v5\n\t" \
"v_lshl_add_u32 v6, v2, 3, v3\n\t" \
"v_lshl_add_u32 v5, v6, 8, v5\n\t" \
"v_and_b32 v6, 1, v4\n\t" \
"v_lshl_add_u32 v5, v6, 3, v5\n\t" \
"v_add_u32 v136, 0x4000, v5\n\t" \
"v_xor_b32 v137, 32, v136\n\t" \
"v_lshrrev_b32 v5, 1, v1\n\t" \
"v_xor_b32 v5, v5, v2\n\t" \
"v_lshlrev_b32 v5, 4, v5\n\t" \
"v_lshl_add_u32 v138, v1, 7, v5\n\t" \
"v_xor_b32 v139, 64, v138\n\t" \
"v_add_u32 v140, 0x10000, v136\n\t" \
"v_add_u32 v141, 0x10000, v137\n\t" \
"v_add_u32 v142, 0x10000, v138\n\t" \
"v_add_u32 v143, 0x10000, v139\n\t" \
"v_lshrrev_b32 v5, 1, v2\n\t" \
"v_or_b32 v5, s60, v5\n\t" \
"v_xor_b32 v5, v5, v1\n\t" \
"v_lshlrev_b32 v5, 4, v5\n\t" \
"v_lshl_add_u32 v5, v1, 8, v5\n\t" \
"v_and_b32 v6, 1, v2\n\t" \
"v_lshl_add_u32 v156, v6, 3, v5\n\t" \
"v_xor_b32 v157, 32, v156\n\t" \
"s_mov_b32 s80, 0x3e0293ee\n\t" \
"s_cmp_eq_u32 s74, 0\n\t" \
"s_cbranch_scc0 L_mkkv_%=\n\t" \
"v_lshrrev_b32 v5, 3, v0\n\t" \
"v_lshlrev_b32 v5, 11, v5\n\t" \
"v_and_b32 v6, 7, v0\n\t" \
"v_xor_b32 v6, v6, v2\n\t" \
"v_lshl_add_u32 v5, v6, 4, v5\n\t" \
"s_mul_i32 s60, s71, 114688\n\t" \
"v_add_u32 v5, s60, v5\n\t" \
"s_lshl_b32 s61, s71, 6\n\t" \
"v_xor_b32 v232, s61, v5\n\t" \
"v_xor_b32 v5, 64, v232\n\t" \
"v_add_u32 v233, 15360, v5\n\t" \
"v_add_u32 v234, 30720, v232\n\t" \
"v_add_u32 v235, 46080, v5\n\t" \
"v_lshlrev_b32 v5, 1, v2\n\t" \
"v_xor_b32 v5, v5, v1\n\t" \
"v_lshlrev_b32 v5, 4, v5\n\t" \
"v_lshl_add_u32 v5, v2, 13, v5\n\t" \
"s_lshl_b32 s60, s71, 18\n\t" \
"v_add_u32 v236, s60, v5\n\t" \
"v_xor_b32 v5, 0x80, v236\n\t" \
"v_add_u32 v237, 31744, v236\n\t" \
"v_add_u32 v238, 63488, v5\n\t" \
"v_add_u32 v239, 95232, v5\n\t" \
"s_mov_b32 s75, 99\n\t" \
"s_mov_b32 s76, 99\n\t" \
"s_mov_b32 s77, 99\n\t" \
"s_branch L_mkdone_%=\n\t" \
"L_mkkv_%=:\n\t" \
"s_xor_b32 s60, s78, 0x2000\n\t" \
"v_lshlrev_b32 v5, 8, v2\n\t" \
"v_add_u32 v6, s60, v5\n\t" \
"v_add_u32 v7, 0, v2\n\t" \
"v_xor_b32 v7, v7, v1\n\t" \
"v_lshl_add_u32 v232, v7, 4, v6\n\t" \
"v_add_u32 v7, 4, v2\n\t" \
"v_xor_b32 v7, v7, v1\n\t" \
"v_lshl_add_u32 v233, v7, 4, v6\n\t" \
"v_add_u32 v7, 8, v2\n\t" \
"v_xor_b32 v7, v7, v1\n\t" \
"v_lshl_add_u32 v234, v7, 4, v6\n\t" \
"v_add_u32 v7, 12, v2\n\t" \
"v_xor_b32 v7, v7, v1\n\t" \
"v_lshl_add_u32 v235, v7, 4, v6\n\t" \
"v_add_u32 v6, s78, v5\n\t" \
"v_lshlrev_b32 v7, 1, v2\n\t" \
"v_xor_b32 v7, v7, v1\n\t" \
"v_lshl_add_u32 v236, v7, 4, v6\n\t" \
"v_xor_b32 v237, 0x80, v236\n\t" \
"s_add_u32 s75, s34, 3\n\t" \
"s_add_u32 s76, s34, 8\n\t" \
"s_mov_b32 s77, 14\n\t" \
"L_mkdone_%=:\n\t" \
"s_barrier\n\t" \
"buffer_load_dwordx2 v[252:253], v254, s[24:27], 0 offen sc1\n\t" \
"ds_read_b64_tr_b16 v[64:65], v136 offset:32768\n\t" \
"ds_read_b64_tr_b16 v[66:67], v136 offset:33792\n\t" \
"ds_read_b64_tr_b16 v[68:69], v137 offset:32768\n\t" \
"ds_read_b64_tr_b16 v[70:71], v137 offset:33792\n\t" \
"ds_read_b128 v[80:83], v138 offset:32768\n\t" \
"ds_read_b128 v[84:87], v138 offset:34816\n\t" \
"ds_read_b128 v[88:91], v138 offset:36864\n\t" \
"ds_read_b128 v[92:95], v138 offset:38912\n\t" \
"ds_read_b128 v[96:99], v138 offset:40960\n\t" \
"ds_read_b128 v[100:103], v138 offset:43008\n\t" \
"ds_read_b128 v[104:107], v138 offset:45056\n\t" \
"s_waitcnt lgkmcnt(0)\n\t" \
"v_mfma_f32_16x16x32_bf16 v[8:11], v[64:67], v[80:83], 0\n\t" \
"ds_read_b64_tr_b16 v[72:73], v136 offset:40960\n\t" \
"ds_read_b64_tr_b16 v[74:75], v136 offset:41984\n\t" \
"v_mfma_f32_16x16x32_bf16 v[36:39], v[68:71], v[80:83], 0\n\t" \
"ds_read_b64_tr_b16 v[76:77], v137 offset:40960\n\t" \
"ds_read_b64_tr_b16 v[78:79], v137 offset:41984\n\t" \
"v_mfma_f32_16x16x32_bf16 v[12:15], v[64:67], v[84:87], 0\n\t" \
"ds_read_b128 v[108:111], v139 offset:32768\n\t" \
"v_mfma_f32_16x16x32_bf16 v[40:43], v[68:71], v[84:87], 0\n\t" \
"ds_read_b128 v[112:115], v139 offset:34816\n\t" \
"v_mfma_f32_16x16x32_bf16 v[16:19], v[64:67], v[88:91], 0\n\t" \
"ds_read_b128 v[116:119], v139 offset:36864\n\t" \
"v_mfma_f32_16x16x32_bf16 v[44:47], v[68:71], v[88:91], 0\n\t" \
"ds_read_b128 v[120:123], v139 offset:38912\n\t" \
"v_mfma_f32_16x16x32_bf16 v[20:23], v[64:67], v[92:95], 0\n\t" \
"ds_read_b128 v[124:127], v139 offset:40960\n\t" \
"v_mfma_f32_16x16x32_bf16 v[48:51], v[68:71], v[92:95], 0\n\t" \
"ds_read_b128 v[128:131], v139 offset:43008\n\t" \
"v_mfma_f32_16x16x32_bf16 v[24:27], v[64:67], v[96:99], 0\n\t" \
"ds_read_b128 v[132:135], v139 offset:45056\n\t" \
"v_mfma_f32_16x16x32_bf16 v[52:55], v[68:71], v[96:99], 0\n\t" \
"v_mfma_f32_16x16x32_bf16 v[28:31], v[64:67], v[100:103], 0\n\t" \
"v_mfma_f32_16x16x32_bf16 v[56:59], v[68:71], v[100:103], 0\n\t" \
"v_mfma_f32_16x16x32_bf16 v[32:35], v[64:67], v[104:107], 0\n\t" \
"v_mfma_f32_16x16x32_bf16 v[60:63], v[68:71], v[104:107], 0\n\t" \
"s_mov_b32 s70, 1\n\t" \
"L_p0g_%=:\n\t" \
"s_waitcnt lgkmcnt(0)\n\t" \
"s_barrier\n\t" \
"s_cmp_eq_u32 s70, s75\n\t" \
"s_cbranch_scc0 L_e021_%=\n\t" \
"s_cmp_eq_u32 s71, 0\n\t" \
"s_cselect_b32 s60, 0x40000000, 0\n\t" \
"v_or_b32 v240, s60, v232\n\t" \
"v_or_b32 v241, s60, v233\n\t" \
"v_or_b32 v242, s60, v234\n\t" \
"v_or_b32 v243, s60, v235\n\t" \
"s_mov_b32 s61, 0\n\t" \
"s_lshr_b32 s63, s61, 4\n\t" \
"s_mul_i32 s63, s63, 0x100000\n\t" \
"s_add_u32 s61, s61, s81\n\t" \
"s_and_b32 s61, s61, 15\n\t" \
"s_lshl_b32 s61, s61, 14\n\t" \
"s_add_u32 s61, s61, s63\n\t" \
"s_add_u32 m0, s78, 0x20000\n\t" \
"s_nop 0\n\t" \
"buffer_load_dwordx4 v240, s[12:15], s61 offen offset:0 nt lds\n\t" \
"buffer_load_dwordx4 v241, s[12:15], s61 offen offset:1024 nt lds\n\t" \
"buffer_load_dwordx4 v242, s[12:15], s61 offen offset:2048 nt lds\n\t" \
"buffer_load_dwordx4 v243, s[12:15], s61 offen offset:3072 nt lds\n\t" \
"s_add_u32 m0, m0, 4096\n\t" \
"s_add_u32 s61, s61, 4096\n\t" \
"buffer_load_dwordx4 v240, s[12:15], s61 offen offset:0 nt lds\n\t" \
"buffer_load_dwordx4 v241, s[12:15], s61 offen offset:1024 nt lds\n\t" \
"buffer_load_dwordx4 v242, s[12:15], s61 offen offset:2048 nt lds\n\t" \
"buffer_load_dwordx4 v243, s[12:15], s61 offen offset:3072 nt lds\n\t" \
"s_add_u32 s61, s71, 0\n\t" \
"s_lshr_b32 s63, s61, 4\n\t" \
"s_mul_i32 s63, s63, 0x100000\n\t" \
"s_add_u32 s61, s61, s81\n\t" \
"s_and_b32 s61, s61, 15\n\t" \
"s_lshl_b32 s61, s61, 14\n\t" \
"s_add_u32 s61, s61, s63\n\t" \
"s_add_u32 m0, s78, 0x18000\n\t" \
"s_nop 0\n\t" \
"buffer_load_dwordx4 v232, s[12:15], s61 offen offset:0 nt lds\n\t" \
"buffer_load_dwordx4 v233, s[12:15], s61 offen offset:1024 nt lds\n\t" \
"buffer_load_dwordx4 v234, s[12:15], s61 offen offset:2048 nt lds\n\t" \
"buffer_load_dwordx4 v235, s[12:15], s61 offen offset:3072 nt lds\n\t" \
"s_add_u32 m0, m0, 4096\n\t" \
"s_add_u32 s61, s61, 4096\n\t" \
"buffer_load_dwordx4 v232, s[12:15], s61 offen offset:0 nt lds\n\t" \
"buffer_load_dwordx4 v233, s[12:15], s61 offen offset:1024 nt lds\n\t" \
"buffer_load_dwordx4 v234, s[12:15], s61 offen offset:2048 nt lds\n\t" \
"buffer_load_dwordx4 v235, s[12:15], s61 offen offset:3072 nt lds\n\t" \
"s_mov_b32 s61, 0\n\t" \
"s_lshr_b32 s63, s61, 4\n\t" \
"s_mul_i32 s63, s63, 0x100000\n\t" \
"s_add_u32 s61, s61, s81\n\t" \
"s_and_b32 s61, s61, 15\n\t" \
"s_lshl_b32 s61, s61, 14\n\t" \
"s_add_u32 s61, s61, s63\n\t" \
"s_add_u32 m0, s73, 0x18000\n\t" \
"s_nop 0\n\t" \
"buffer_load_dwordx4 v236, s[16:19], s61 offen offset:0 nt lds\n\t" \
"buffer_load_dwordx4 v237, s[16:19], s61 offen offset:1024 nt lds\n\t" \
"buffer_load_dwordx4 v236, s[16:19], s61 offen offset:2048 nt lds\n\t" \
"buffer_load_dwordx4 v237, s[16:19], s61 offen offset:3072 nt lds\n\t" \
"s_add_u32 m0, m0, 4096\n\t" \
"s_add_u32 s61, s61, 4096\n\t" \
"buffer_load_dwordx4 v236, s[16:19], s61 offen offset:0 nt lds\n\t" \
"buffer_load_dwordx4 v237, s[16:19], s61 offen offset:1024 nt lds\n\t" \
"buffer_load_dwordx4 v236, s[16:19], s61 offen offset:2048 nt lds\n\t" \
"buffer_load_dwordx4 v237, s[16:19], s61 offen offset:3072 nt lds\n\t" \
"L_e021_%=:\n\t" \
"s_cmp_eq_u32 s70, s76\n\t" \
"s_cbranch_scc0 L_e121_%=\n\t" \
"s_mov_b32 s61, 2\n\t" \
"s_lshr_b32 s63, s61, 4\n\t" \
"s_mul_i32 s63, s63, 0x100000\n\t" \
"s_add_u32 s61, s61, s81\n\t" \
"s_and_b32 s61, s61, 15\n\t" \
"s_lshl_b32 s61, s61, 14\n\t" \
"s_add_u32 s61, s61, s63\n\t" \
"s_add_u32 m0, s73, 0x20000\n\t" \
"s_nop 0\n\t" \
"buffer_load_dwordx4 v236, s[16:19], s61 offen offset:0 nt lds\n\t" \
"buffer_load_dwordx4 v237, s[16:19], s61 offen offset:1024 nt lds\n\t" \
"buffer_load_dwordx4 v236, s[16:19], s61 offen offset:2048 nt lds\n\t" \
"buffer_load_dwordx4 v237, s[16:19], s61 offen offset:3072 nt lds\n\t" \
"s_add_u32 m0, m0, 4096\n\t" \
"s_add_u32 s61, s61, 4096\n\t" \
"buffer_load_dwordx4 v236, s[16:19], s61 offen offset:0 nt lds\n\t" \
"buffer_load_dwordx4 v237, s[16:19], s61 offen offset:1024 nt lds\n\t" \
"buffer_load_dwordx4 v236, s[16:19], s61 offen offset:2048 nt lds\n\t" \
"buffer_load_dwordx4 v237, s[16:19], s61 offen offset:3072 nt lds\n\t" \
"L_e121_%=:\n\t" \
"v_mfma_f32_16x16x32_bf16 v[8:11], v[72:75], v[108:111], v[8:11]\n\t" \
"ds_read_b64_tr_b16 v[64:65], v140 offset:0\n\t" \
"ds_read_b64_tr_b16 v[66:67], v140 offset:1024\n\t" \
"v_mfma_f32_16x16x32_bf16 v[36:39], v[76:79], v[108:111], v[36:39]\n\t" \
"ds_read_b64_tr_b16 v[68:69], v141 offset:0\n\t" \
"ds_read_b64_tr_b16 v[70:71], v141 offset:1024\n\t" \
"v_mfma_f32_16x16x32_bf16 v[12:15], v[72:75], v[112:115], v[12:15]\n\t" \
"ds_read_b128 v[80:83], v142 offset:0\n\t" \
"v_mfma_f32_16x16x32_bf16 v[40:43], v[76:79], v[112:115], v[40:43]\n\t" \
"ds_read_b128 v[84:87], v142 offset:2048\n\t" \
"v_mfma_f32_16x16x32_bf16 v[16:19], v[72:75], v[116:119], v[16:19]\n\t" \
"ds_read_b128 v[88:91], v142 offset:4096\n\t" \
"v_mfma_f32_16x16x32_bf16 v[44:47], v[76:79], v[116:119], v[44:47]\n\t" \
"ds_read_b128 v[92:95], v142 offset:6144\n\t" \
"v_mfma_f32_16x16x32_bf16 v[20:23], v[72:75], v[120:123], v[20:23]\n\t" \
"ds_read_b128 v[96:99], v142 offset:8192\n\t" \
"v_mfma_f32_16x16x32_bf16 v[48:51], v[76:79], v[120:123], v[48:51]\n\t" \
"ds_read_b128 v[100:103], v142 offset:10240\n\t" \
"v_mfma_f32_16x16x32_bf16 v[24:27], v[72:75], v[124:127], v[24:27]\n\t" \
"ds_read_b128 v[104:107], v142 offset:12288\n\t" \
"v_mfma_f32_16x16x32_bf16 v[52:55], v[76:79], v[124:127], v[52:55]\n\t" \
"v_mfma_f32_16x16x32_bf16 v[28:31], v[72:75], v[128:131], v[28:31]\n\t" \
"v_mfma_f32_16x16x32_bf16 v[56:59], v[76:79], v[128:131], v[56:59]\n\t" \
"v_mfma_f32_16x16x32_bf16 v[32:35], v[72:75], v[132:135], v[32:35]\n\t" \
"v_mfma_f32_16x16x32_bf16 v[60:63], v[76:79], v[132:135], v[60:63]\n\t" \
"s_waitcnt lgkmcnt(0)\n\t" \
"v_mfma_f32_16x16x32_bf16 v[8:11], v[64:67], v[80:83], v[8:11]\n\t" \
"ds_read_b64_tr_b16 v[72:73], v140 offset:8192\n\t" \
"ds_read_b64_tr_b16 v[74:75], v140 offset:9216\n\t" \
"v_mfma_f32_16x16x32_bf16 v[36:39], v[68:71], v[80:83], v[36:39]\n\t" \
"ds_read_b64_tr_b16 v[76:77], v141 offset:8192\n\t" \
"ds_read_b64_tr_b16 v[78:79], v141 offset:9216\n\t" \
"v_mfma_f32_16x16x32_bf16 v[12:15], v[64:67], v[84:87], v[12:15]\n\t" \
"ds_read_b128 v[108:111], v143 offset:0\n\t" \
"v_mfma_f32_16x16x32_bf16 v[40:43], v[68:71], v[84:87], v[40:43]\n\t" \
"ds_read_b128 v[112:115], v143 offset:2048\n\t" \
"v_mfma_f32_16x16x32_bf16 v[16:19], v[64:67], v[88:91], v[16:19]\n\t" \
"ds_read_b128 v[116:119], v143 offset:4096\n\t" \
"v_mfma_f32_16x16x32_bf16 v[44:47], v[68:71], v[88:91], v[44:47]\n\t" \
"ds_read_b128 v[120:123], v143 offset:6144\n\t" \
"v_mfma_f32_16x16x32_bf16 v[20:23], v[64:67], v[92:95], v[20:23]\n\t" \
"ds_read_b128 v[124:127], v143 offset:8192\n\t" \
"v_mfma_f32_16x16x32_bf16 v[48:51], v[68:71], v[92:95], v[48:51]\n\t" \
"ds_read_b128 v[128:131], v143 offset:10240\n\t" \
"v_mfma_f32_16x16x32_bf16 v[24:27], v[64:67], v[96:99], v[24:27]\n\t" \
"ds_read_b128 v[132:135], v143 offset:12288\n\t" \
"v_mfma_f32_16x16x32_bf16 v[52:55], v[68:71], v[96:99], v[52:55]\n\t" \
"v_mfma_f32_16x16x32_bf16 v[28:31], v[64:67], v[100:103], v[28:31]\n\t" \
"v_mfma_f32_16x16x32_bf16 v[56:59], v[68:71], v[100:103], v[56:59]\n\t" \
"v_mfma_f32_16x16x32_bf16 v[32:35], v[64:67], v[104:107], v[32:35]\n\t" \
"v_mfma_f32_16x16x32_bf16 v[60:63], v[68:71], v[104:107], v[60:63]\n\t" \
"s_add_u32 s70, s70, 1\n\t" \
"s_waitcnt lgkmcnt(0)\n\t" \
"s_barrier\n\t" \
"s_cmp_eq_u32 s70, s75\n\t" \
"s_cbranch_scc0 L_e022_%=\n\t" \
"s_cmp_eq_u32 s71, 0\n\t" \
"s_cselect_b32 s60, 0x40000000, 0\n\t" \
"v_or_b32 v240, s60, v232\n\t" \
"v_or_b32 v241, s60, v233\n\t" \
"v_or_b32 v242, s60, v234\n\t" \
"v_or_b32 v243, s60, v235\n\t" \
"s_mov_b32 s61, 0\n\t" \
"s_lshr_b32 s63, s61, 4\n\t" \
"s_mul_i32 s63, s63, 0x100000\n\t" \
"s_add_u32 s61, s61, s81\n\t" \
"s_and_b32 s61, s61, 15\n\t" \
"s_lshl_b32 s61, s61, 14\n\t" \
"s_add_u32 s61, s61, s63\n\t" \
"s_add_u32 m0, s78, 0x20000\n\t" \
"s_nop 0\n\t" \
"buffer_load_dwordx4 v240, s[12:15], s61 offen offset:0 nt lds\n\t" \
"buffer_load_dwordx4 v241, s[12:15], s61 offen offset:1024 nt lds\n\t" \
"buffer_load_dwordx4 v242, s[12:15], s61 offen offset:2048 nt lds\n\t" \
"buffer_load_dwordx4 v243, s[12:15], s61 offen offset:3072 nt lds\n\t" \
"s_add_u32 m0, m0, 4096\n\t" \
"s_add_u32 s61, s61, 4096\n\t" \
"buffer_load_dwordx4 v240, s[12:15], s61 offen offset:0 nt lds\n\t" \
"buffer_load_dwordx4 v241, s[12:15], s61 offen offset:1024 nt lds\n\t" \
"buffer_load_dwordx4 v242, s[12:15], s61 offen offset:2048 nt lds\n\t" \
"buffer_load_dwordx4 v243, s[12:15], s61 offen offset:3072 nt lds\n\t" \
"s_add_u32 s61, s71, 0\n\t" \
"s_lshr_b32 s63, s61, 4\n\t" \
"s_mul_i32 s63, s63, 0x100000\n\t" \
"s_add_u32 s61, s61, s81\n\t" \
"s_and_b32 s61, s61, 15\n\t" \
"s_lshl_b32 s61, s61, 14\n\t" \
"s_add_u32 s61, s61, s63\n\t" \
"s_add_u32 m0, s78, 0x18000\n\t" \
"s_nop 0\n\t" \
"buffer_load_dwordx4 v232, s[12:15], s61 offen offset:0 nt lds\n\t" \
"buffer_load_dwordx4 v233, s[12:15], s61 offen offset:1024 nt lds\n\t" \
"buffer_load_dwordx4 v234, s[12:15], s61 offen offset:2048 nt lds\n\t" \
"buffer_load_dwordx4 v235, s[12:15], s61 offen offset:3072 nt lds\n\t" \
"s_add_u32 m0, m0, 4096\n\t" \
"s_add_u32 s61, s61, 4096\n\t" \
"buffer_load_dwordx4 v232, s[12:15], s61 offen offset:0 nt lds\n\t" \
"buffer_load_dwordx4 v233, s[12:15], s61 offen offset:1024 nt lds\n\t" \
"buffer_load_dwordx4 v234, s[12:15], s61 offen offset:2048 nt lds\n\t" \
"buffer_load_dwordx4 v235, s[12:15], s61 offen offset:3072 nt lds\n\t" \
"s_mov_b32 s61, 0\n\t" \
"s_lshr_b32 s63, s61, 4\n\t" \
"s_mul_i32 s63, s63, 0x100000\n\t" \
"s_add_u32 s61, s61, s81\n\t" \
"s_and_b32 s61, s61, 15\n\t" \
"s_lshl_b32 s61, s61, 14\n\t" \
"s_add_u32 s61, s61, s63\n\t" \
"s_add_u32 m0, s73, 0x18000\n\t" \
"s_nop 0\n\t" \
"buffer_load_dwordx4 v236, s[16:19], s61 offen offset:0 nt lds\n\t" \
"buffer_load_dwordx4 v237, s[16:19], s61 offen offset:1024 nt lds\n\t" \
"buffer_load_dwordx4 v236, s[16:19], s61 offen offset:2048 nt lds\n\t" \
"buffer_load_dwordx4 v237, s[16:19], s61 offen offset:3072 nt lds\n\t" \
"s_add_u32 m0, m0, 4096\n\t" \
"s_add_u32 s61, s61, 4096\n\t" \
"buffer_load_dwordx4 v236, s[16:19], s61 offen offset:0 nt lds\n\t" \
"buffer_load_dwordx4 v237, s[16:19], s61 offen offset:1024 nt lds\n\t" \
"buffer_load_dwordx4 v236, s[16:19], s61 offen offset:2048 nt lds\n\t" \
"buffer_load_dwordx4 v237, s[16:19], s61 offen offset:3072 nt lds\n\t" \
"L_e022_%=:\n\t" \
"s_cmp_eq_u32 s70, s76\n\t" \
"s_cbranch_scc0 L_e122_%=\n\t" \
"s_mov_b32 s61, 2\n\t" \
"s_lshr_b32 s63, s61, 4\n\t" \
"s_mul_i32 s63, s63, 0x100000\n\t" \
"s_add_u32 s61, s61, s81\n\t" \
"s_and_b32 s61, s61, 15\n\t" \
"s_lshl_b32 s61, s61, 14\n\t" \
"s_add_u32 s61, s61, s63\n\t" \
"s_add_u32 m0, s73, 0x20000\n\t" \
"s_nop 0\n\t" \
"buffer_load_dwordx4 v236, s[16:19], s61 offen offset:0 nt lds\n\t" \
"buffer_load_dwordx4 v237, s[16:19], s61 offen offset:1024 nt lds\n\t" \
"buffer_load_dwordx4 v236, s[16:19], s61 offen offset:2048 nt lds\n\t" \
"buffer_load_dwordx4 v237, s[16:19], s61 offen offset:3072 nt lds\n\t" \
"s_add_u32 m0, m0, 4096\n\t" \
"s_add_u32 s61, s61, 4096\n\t" \
"buffer_load_dwordx4 v236, s[16:19], s61 offen offset:0 nt lds\n\t" \
"buffer_load_dwordx4 v237, s[16:19], s61 offen offset:1024 nt lds\n\t" \
"buffer_load_dwordx4 v236, s[16:19], s61 offen offset:2048 nt lds\n\t" \
"buffer_load_dwordx4 v237, s[16:19], s61 offen offset:3072 nt lds\n\t" \
"L_e122_%=:\n\t" \
"s_cmp_eq_u32 s70, s77\n\t" \
"s_cbranch_scc0 L_e222_%=\n\t" \
"s_add_u32 s61, s71, 1\n\t" \
"s_lshr_b32 s63, s61, 4\n\t" \
"s_mul_i32 s63, s63, 0x100000\n\t" \
"s_add_u32 s61, s61, s81\n\t" \
"s_and_b32 s61, s61, 15\n\t" \
"s_lshl_b32 s61, s61, 14\n\t" \
"s_add_u32 s61, s61, s63\n\t" \
"s_add_u32 m0, s78, 0x10000\n\t" \
"s_nop 0\n\t" \
"buffer_load_dwordx4 v232, s[12:15], s61 offen offset:0 nt lds\n\t" \
"buffer_load_dwordx4 v233, s[12:15], s61 offen offset:1024 nt lds\n\t" \
"buffer_load_dwordx4 v234, s[12:15], s61 offen offset:2048 nt lds\n\t" \
"buffer_load_dwordx4 v235, s[12:15], s61 offen offset:3072 nt lds\n\t" \
"s_add_u32 m0, m0, 4096\n\t" \
"s_add_u32 s61, s61, 4096\n\t" \
"buffer_load_dwordx4 v232, s[12:15], s61 offen offset:0 nt lds\n\t" \
"buffer_load_dwordx4 v233, s[12:15], s61 offen offset:1024 nt lds\n\t" \
"buffer_load_dwordx4 v234, s[12:15], s61 offen offset:2048 nt lds\n\t" \
"buffer_load_dwordx4 v235, s[12:15], s61 offen offset:3072 nt lds\n\t" \
"s_mov_b32 s61, 1\n\t" \
"s_lshr_b32 s63, s61, 4\n\t" \
"s_mul_i32 s63, s63, 0x100000\n\t" \
"s_add_u32 s61, s61, s81\n\t" \
"s_and_b32 s61, s61, 15\n\t" \
"s_lshl_b32 s61, s61, 14\n\t" \
"s_add_u32 s61, s61, s63\n\t" \
"s_add_u32 m0, s73, 0x10000\n\t" \
"s_nop 0\n\t" \
"buffer_load_dwordx4 v236, s[16:19], s61 offen offset:0 nt lds\n\t" \
"buffer_load_dwordx4 v237, s[16:19], s61 offen offset:1024 nt lds\n\t" \
"buffer_load_dwordx4 v236, s[16:19], s61 offen offset:2048 nt lds\n\t" \
"buffer_load_dwordx4 v237, s[16:19], s61 offen offset:3072 nt lds\n\t" \
"s_add_u32 m0, m0, 4096\n\t" \
"s_add_u32 s61, s61, 4096\n\t" \
"buffer_load_dwordx4 v236, s[16:19], s61 offen offset:0 nt lds\n\t" \
"buffer_load_dwordx4 v237, s[16:19], s61 offen offset:1024 nt lds\n\t" \
"buffer_load_dwordx4 v236, s[16:19], s61 offen offset:2048 nt lds\n\t" \
"buffer_load_dwordx4 v237, s[16:19], s61 offen offset:3072 nt lds\n\t" \
"L_e222_%=:\n\t" \
"v_mfma_f32_16x16x32_bf16 v[8:11], v[72:75], v[108:111], v[8:11]\n\t" \
"ds_read_b64_tr_b16 v[64:65], v136 offset:0\n\t" \
"ds_read_b64_tr_b16 v[66:67], v136 offset:1024\n\t" \
"v_mfma_f32_16x16x32_bf16 v[36:39], v[76:79], v[108:111], v[36:39]\n\t" \
"ds_read_b64_tr_b16 v[68:69], v137 offset:0\n\t" \
"ds_read_b64_tr_b16 v[70:71], v137 offset:1024\n\t" \
"v_mfma_f32_16x16x32_bf16 v[12:15], v[72:75], v[112:115], v[12:15]\n\t" \
"ds_read_b128 v[80:83], v138 offset:0\n\t" \
"v_mfma_f32_16x16x32_bf16 v[40:43], v[76:79], v[112:115], v[40:43]\n\t" \
"ds_read_b128 v[84:87], v138 offset:2048\n\t" \
"v_mfma_f32_16x16x32_bf16 v[16:19], v[72:75], v[116:119], v[16:19]\n\t" \
"ds_read_b128 v[88:91], v138 offset:4096\n\t" \
"v_mfma_f32_16x16x32_bf16 v[44:47], v[76:79], v[116:119], v[44:47]\n\t" \
"ds_read_b128 v[92:95], v138 offset:6144\n\t" \
"v_mfma_f32_16x16x32_bf16 v[20:23], v[72:75], v[120:123], v[20:23]\n\t" \
"ds_read_b128 v[96:99], v138 offset:8192\n\t" \
"v_mfma_f32_16x16x32_bf16 v[48:51], v[76:79], v[120:123], v[48:51]\n\t" \
"ds_read_b128 v[100:103], v138 offset:10240\n\t" \
"v_mfma_f32_16x16x32_bf16 v[24:27], v[72:75], v[124:127], v[24:27]\n\t" \
"ds_read_b128 v[104:107], v138 offset:12288\n\t" \
"v_mfma_f32_16x16x32_bf16 v[52:55], v[76:79], v[124:127], v[52:55]\n\t" \
"v_mfma_f32_16x16x32_bf16 v[28:31], v[72:75], v[128:131], v[28:31]\n\t" \
"v_mfma_f32_16x16x32_bf16 v[56:59], v[76:79], v[128:131], v[56:59]\n\t" \
"v_mfma_f32_16x16x32_bf16 v[32:35], v[72:75], v[132:135], v[32:35]\n\t" \
"v_mfma_f32_16x16x32_bf16 v[60:63], v[76:79], v[132:135], v[60:63]\n\t" \
"s_waitcnt lgkmcnt(0)\n\t" \
"v_mfma_f32_16x16x32_bf16 v[8:11], v[64:67], v[80:83], v[8:11]\n\t" \
"ds_read_b64_tr_b16 v[72:73], v136 offset:8192\n\t" \
"ds_read_b64_tr_b16 v[74:75], v136 offset:9216\n\t" \
"v_mfma_f32_16x16x32_bf16 v[36:39], v[68:71], v[80:83], v[36:39]\n\t" \
"ds_read_b64_tr_b16 v[76:77], v137 offset:8192\n\t" \
"ds_read_b64_tr_b16 v[78:79], v137 offset:9216\n\t" \
"v_mfma_f32_16x16x32_bf16 v[12:15], v[64:67], v[84:87], v[12:15]\n\t" \
"ds_read_b128 v[108:111], v139 offset:0\n\t" \
"v_mfma_f32_16x16x32_bf16 v[40:43], v[68:71], v[84:87], v[40:43]\n\t" \
"ds_read_b128 v[112:115], v139 offset:2048\n\t" \
"v_mfma_f32_16x16x32_bf16 v[16:19], v[64:67], v[88:91], v[16:19]\n\t" \
"ds_read_b128 v[116:119], v139 offset:4096\n\t" \
"v_mfma_f32_16x16x32_bf16 v[44:47], v[68:71], v[88:91], v[44:47]\n\t" \
"ds_read_b128 v[120:123], v139 offset:6144\n\t" \
"v_mfma_f32_16x16x32_bf16 v[20:23], v[64:67], v[92:95], v[20:23]\n\t" \
"ds_read_b128 v[124:127], v139 offset:8192\n\t" \
"v_mfma_f32_16x16x32_bf16 v[48:51], v[68:71], v[92:95], v[48:51]\n\t" \
"ds_read_b128 v[128:131], v139 offset:10240\n\t" \
"v_mfma_f32_16x16x32_bf16 v[24:27], v[64:67], v[96:99], v[24:27]\n\t" \
"ds_read_b128 v[132:135], v139 offset:12288\n\t" \
"v_mfma_f32_16x16x32_bf16 v[52:55], v[68:71], v[96:99], v[52:55]\n\t" \
"v_mfma_f32_16x16x32_bf16 v[28:31], v[64:67], v[100:103], v[28:31]\n\t" \
"v_mfma_f32_16x16x32_bf16 v[56:59], v[68:71], v[100:103], v[56:59]\n\t" \
"v_mfma_f32_16x16x32_bf16 v[32:35], v[64:67], v[104:107], v[32:35]\n\t" \
"v_mfma_f32_16x16x32_bf16 v[60:63], v[68:71], v[104:107], v[60:63]\n\t" \
"s_add_u32 s70, s70, 1\n\t" \
"s_waitcnt lgkmcnt(0)\n\t" \
"s_barrier\n\t" \
"s_cmp_eq_u32 s70, s75\n\t" \
"s_cbranch_scc0 L_e023_%=\n\t" \
"s_cmp_eq_u32 s71, 0\n\t" \
"s_cselect_b32 s60, 0x40000000, 0\n\t" \
"v_or_b32 v240, s60, v232\n\t" \
"v_or_b32 v241, s60, v233\n\t" \
"v_or_b32 v242, s60, v234\n\t" \
"v_or_b32 v243, s60, v235\n\t" \
"s_mov_b32 s61, 0\n\t" \
"s_lshr_b32 s63, s61, 4\n\t" \
"s_mul_i32 s63, s63, 0x100000\n\t" \
"s_add_u32 s61, s61, s81\n\t" \
"s_and_b32 s61, s61, 15\n\t" \
"s_lshl_b32 s61, s61, 14\n\t" \
"s_add_u32 s61, s61, s63\n\t" \
"s_add_u32 m0, s78, 0x20000\n\t" \
"s_nop 0\n\t" \
"buffer_load_dwordx4 v240, s[12:15], s61 offen offset:0 nt lds\n\t" \
"buffer_load_dwordx4 v241, s[12:15], s61 offen offset:1024 nt lds\n\t" \
"buffer_load_dwordx4 v242, s[12:15], s61 offen offset:2048 nt lds\n\t" \
"buffer_load_dwordx4 v243, s[12:15], s61 offen offset:3072 nt lds\n\t" \
"s_add_u32 m0, m0, 4096\n\t" \
"s_add_u32 s61, s61, 4096\n\t" \
"buffer_load_dwordx4 v240, s[12:15], s61 offen offset:0 nt lds\n\t" \
"buffer_load_dwordx4 v241, s[12:15], s61 offen offset:1024 nt lds\n\t" \
"buffer_load_dwordx4 v242, s[12:15], s61 offen offset:2048 nt lds\n\t" \
"buffer_load_dwordx4 v243, s[12:15], s61 offen offset:3072 nt lds\n\t" \
"s_add_u32 s61, s71, 0\n\t" \
"s_lshr_b32 s63, s61, 4\n\t" \
"s_mul_i32 s63, s63, 0x100000\n\t" \
"s_add_u32 s61, s61, s81\n\t" \
"s_and_b32 s61, s61, 15\n\t" \
"s_lshl_b32 s61, s61, 14\n\t" \
"s_add_u32 s61, s61, s63\n\t" \
"s_add_u32 m0, s78, 0x18000\n\t" \
"s_nop 0\n\t" \
"buffer_load_dwordx4 v232, s[12:15], s61 offen offset:0 nt lds\n\t" \
"buffer_load_dwordx4 v233, s[12:15], s61 offen offset:1024 nt lds\n\t" \
"buffer_load_dwordx4 v234, s[12:15], s61 offen offset:2048 nt lds\n\t" \
"buffer_load_dwordx4 v235, s[12:15], s61 offen offset:3072 nt lds\n\t" \
"s_add_u32 m0, m0, 4096\n\t" \
"s_add_u32 s61, s61, 4096\n\t" \
"buffer_load_dwordx4 v232, s[12:15], s61 offen offset:0 nt lds\n\t" \
"buffer_load_dwordx4 v233, s[12:15], s61 offen offset:1024 nt lds\n\t" \
"buffer_load_dwordx4 v234, s[12:15], s61 offen offset:2048 nt lds\n\t" \
"buffer_load_dwordx4 v235, s[12:15], s61 offen offset:3072 nt lds\n\t" \
"s_mov_b32 s61, 0\n\t" \
"s_lshr_b32 s63, s61, 4\n\t" \
"s_mul_i32 s63, s63, 0x100000\n\t" \
"s_add_u32 s61, s61, s81\n\t" \
"s_and_b32 s61, s61, 15\n\t" \
"s_lshl_b32 s61, s61, 14\n\t" \
"s_add_u32 s61, s61, s63\n\t" \
"s_add_u32 m0, s73, 0x18000\n\t" \
"s_nop 0\n\t" \
"buffer_load_dwordx4 v236, s[16:19], s61 offen offset:0 nt lds\n\t" \
"buffer_load_dwordx4 v237, s[16:19], s61 offen offset:1024 nt lds\n\t" \
"buffer_load_dwordx4 v236, s[16:19], s61 offen offset:2048 nt lds\n\t" \
"buffer_load_dwordx4 v237, s[16:19], s61 offen offset:3072 nt lds\n\t" \
"s_add_u32 m0, m0, 4096\n\t" \
"s_add_u32 s61, s61, 4096\n\t" \
"buffer_load_dwordx4 v236, s[16:19], s61 offen offset:0 nt lds\n\t" \
"buffer_load_dwordx4 v237, s[16:19], s61 offen offset:1024 nt lds\n\t" \
"buffer_load_dwordx4 v236, s[16:19], s61 offen offset:2048 nt lds\n\t" \
"buffer_load_dwordx4 v237, s[16:19], s61 offen offset:3072 nt lds\n\t" \
"L_e023_%=:\n\t" \
"s_cmp_eq_u32 s70, s76\n\t" \
"s_cbranch_scc0 L_e123_%=\n\t" \
"s_mov_b32 s61, 2\n\t" \
"s_lshr_b32 s63, s61, 4\n\t" \
"s_mul_i32 s63, s63, 0x100000\n\t" \
"s_add_u32 s61, s61, s81\n\t" \
"s_and_b32 s61, s61, 15\n\t" \
"s_lshl_b32 s61, s61, 14\n\t" \
"s_add_u32 s61, s61, s63\n\t" \
"s_add_u32 m0, s73, 0x20000\n\t" \
"s_nop 0\n\t" \
"buffer_load_dwordx4 v236, s[16:19], s61 offen offset:0 nt lds\n\t" \
"buffer_load_dwordx4 v237, s[16:19], s61 offen offset:1024 nt lds\n\t" \
"buffer_load_dwordx4 v236, s[16:19], s61 offen offset:2048 nt lds\n\t" \
"buffer_load_dwordx4 v237, s[16:19], s61 offen offset:3072 nt lds\n\t" \
"s_add_u32 m0, m0, 4096\n\t" \
"s_add_u32 s61, s61, 4096\n\t" \
"buffer_load_dwordx4 v236, s[16:19], s61 offen offset:0 nt lds\n\t" \
"buffer_load_dwordx4 v237, s[16:19], s61 offen offset:1024 nt lds\n\t" \
"buffer_load_dwordx4 v236, s[16:19], s61 offen offset:2048 nt lds\n\t" \
"buffer_load_dwordx4 v237, s[16:19], s61 offen offset:3072 nt lds\n\t" \
"L_e123_%=:\n\t" \
"v_mfma_f32_16x16x32_bf16 v[8:11], v[72:75], v[108:111], v[8:11]\n\t" \
"ds_read_b64_tr_b16 v[64:65], v136 offset:32768\n\t" \
"ds_read_b64_tr_b16 v[66:67], v136 offset:33792\n\t" \
"v_mfma_f32_16x16x32_bf16 v[36:39], v[76:79], v[108:111], v[36:39]\n\t" \
"ds_read_b64_tr_b16 v[68:69], v137 offset:32768\n\t" \
"ds_read_b64_tr_b16 v[70:71], v137 offset:33792\n\t" \
"v_mfma_f32_16x16x32_bf16 v[12:15], v[72:75], v[112:115], v[12:15]\n\t" \
"ds_read_b128 v[80:83], v138 offset:32768\n\t" \
"v_mfma_f32_16x16x32_bf16 v[40:43], v[76:79], v[112:115], v[40:43]\n\t" \
"ds_read_b128 v[84:87], v138 offset:34816\n\t" \
"v_mfma_f32_16x16x32_bf16 v[16:19], v[72:75], v[116:119], v[16:19]\n\t" \
"ds_read_b128 v[88:91], v138 offset:36864\n\t" \
"v_mfma_f32_16x16x32_bf16 v[44:47], v[76:79], v[116:119], v[44:47]\n\t" \
"ds_read_b128 v[92:95], v138 offset:38912\n\t" \
"v_mfma_f32_16x16x32_bf16 v[20:23], v[72:75], v[120:123], v[20:23]\n\t" \
"ds_read_b128 v[96:99], v138 offset:40960\n\t" \
"v_mfma_f32_16x16x32_bf16 v[48:51], v[76:79], v[120:123], v[48:51]\n\t" \
"ds_read_b128 v[100:103], v138 offset:43008\n\t" \
"v_mfma_f32_16x16x32_bf16 v[24:27], v[72:75], v[124:127], v[24:27]\n\t" \
"ds_read_b128 v[104:107], v138 offset:45056\n\t" \
"v_mfma_f32_16x16x32_bf16 v[52:55], v[76:79], v[124:127], v[52:55]\n\t" \
"v_mfma_f32_16x16x32_bf16 v[28:31], v[72:75], v[128:131], v[28:31]\n\t" \
"v_mfma_f32_16x16x32_bf16 v[56:59], v[76:79], v[128:131], v[56:59]\n\t" \
"v_mfma_f32_16x16x32_bf16 v[32:35], v[72:75], v[132:135], v[32:35]\n\t" \
"v_mfma_f32_16x16x32_bf16 v[60:63], v[76:79], v[132:135], v[60:63]\n\t" \
"s_waitcnt lgkmcnt(0)\n\t" \
"v_mfma_f32_16x16x32_bf16 v[8:11], v[64:67], v[80:83], v[8:11]\n\t" \
"ds_read_b64_tr_b16 v[72:73], v136 offset:40960\n\t" \
"ds_read_b64_tr_b16 v[74:75], v136 offset:41984\n\t" \
"v_mfma_f32_16x16x32_bf16 v[36:39], v[68:71], v[80:83], v[36:39]\n\t" \
"ds_read_b64_tr_b16 v[76:77], v137 offset:40960\n\t" \
"ds_read_b64_tr_b16 v[78:79], v137 offset:41984\n\t" \
"v_mfma_f32_16x16x32_bf16 v[12:15], v[64:67], v[84:87], v[12:15]\n\t" \
"ds_read_b128 v[108:111], v139 offset:32768\n\t" \
"v_mfma_f32_16x16x32_bf16 v[40:43], v[68:71], v[84:87], v[40:43]\n\t" \
"ds_read_b128 v[112:115], v139 offset:34816\n\t" \
"v_mfma_f32_16x16x32_bf16 v[16:19], v[64:67], v[88:91], v[16:19]\n\t" \
"ds_read_b128 v[116:119], v139 offset:36864\n\t" \
"v_mfma_f32_16x16x32_bf16 v[44:47], v[68:71], v[88:91], v[44:47]\n\t" \
"ds_read_b128 v[120:123], v139 offset:38912\n\t" \
"v_mfma_f32_16x16x32_bf16 v[20:23], v[64:67], v[92:95], v[20:23]\n\t" \
"ds_read_b128 v[124:127], v139 offset:40960\n\t" \
"v_mfma_f32_16x16x32_bf16 v[48:51], v[68:71], v[92:95], v[48:51]\n\t" \
"ds_read_b128 v[128:131], v139 offset:43008\n\t" \
"v_mfma_f32_16x16x32_bf16 v[24:27], v[64:67], v[96:99], v[24:27]\n\t" \
"ds_read_b128 v[132:135], v139 offset:45056\n\t" \
"v_mfma_f32_16x16x32_bf16 v[52:55], v[68:71], v[96:99], v[52:55]\n\t" \
"v_mfma_f32_16x16x32_bf16 v[28:31], v[64:67], v[100:103], v[28:31]\n\t" \
"v_mfma_f32_16x16x32_bf16 v[56:59], v[68:71], v[100:103], v[56:59]\n\t" \
"v_mfma_f32_16x16x32_bf16 v[32:35], v[64:67], v[104:107], v[32:35]\n\t" \
"v_mfma_f32_16x16x32_bf16 v[60:63], v[68:71], v[104:107], v[60:63]\n\t" \
"s_add_u32 s70, s70, 1\n\t" \
"s_cmp_lt_u32 s70, 16\n\t" \
"s_cbranch_scc1 L_p0g_%=\n\t" \
"s_waitcnt lgkmcnt(0)\n\t" \
"v_mov_b32 v6, s80\n\t" \
"v_mfma_f32_16x16x32_bf16 v[8:11], v[72:75], v[108:111], v[8:11]\n\t" \
"v_mfma_f32_16x16x32_bf16 v[36:39], v[76:79], v[108:111], v[36:39]\n\t" \
"v_mfma_f32_16x16x32_bf16 v[12:15], v[72:75], v[112:115], v[12:15]\n\t" \
"v_mfma_f32_16x16x32_bf16 v[40:43], v[76:79], v[112:115], v[40:43]\n\t" \
"s_nop 4\n\t" \
"v_pk_mul_f32 v[8:9], v[8:9], v[6:7] op_sel_hi:[1,0]\n\t" \
"v_pk_mul_f32 v[10:11], v[10:11], v[6:7] op_sel_hi:[1,0]\n\t" \
"v_cvt_pk_bf16_f32 v8, v8, v9\n\t" \
"v_cvt_pk_bf16_f32 v9, v10, v11\n\t" \
"v_mfma_f32_16x16x32_bf16 v[16:19], v[72:75], v[116:119], v[16:19]\n\t" \
"ds_write_b64 v156, v[8:9] offset:0\n\t" \
"v_pk_mul_f32 v[36:37], v[36:37], v[6:7] op_sel_hi:[1,0]\n\t" \
"v_pk_mul_f32 v[38:39], v[38:39], v[6:7] op_sel_hi:[1,0]\n\t" \
"v_cvt_pk_bf16_f32 v36, v36, v37\n\t" \
"v_cvt_pk_bf16_f32 v37, v38, v39\n\t" \
"v_mfma_f32_16x16x32_bf16 v[44:47], v[76:79], v[116:119], v[44:47]\n\t" \
"ds_write_b64 v157, v[36:37] offset:0\n\t" \
"v_pk_mul_f32 v[12:13], v[12:13], v[6:7] op_sel_hi:[1,0]\n\t" \
"v_pk_mul_f32 v[14:15], v[14:15], v[6:7] op_sel_hi:[1,0]\n\t" \
"v_cvt_pk_bf16_f32 v12, v12, v13\n\t" \
"v_cvt_pk_bf16_f32 v13, v14, v15\n\t" \
"v_mfma_f32_16x16x32_bf16 v[20:23], v[72:75], v[120:123], v[20:23]\n\t" \
"ds_write_b64 v156, v[12:13] offset:4096\n\t" \
"v_pk_mul_f32 v[40:41], v[40:41], v[6:7] op_sel_hi:[1,0]\n\t" \
"v_pk_mul_f32 v[42:43], v[42:43], v[6:7] op_sel_hi:[1,0]\n\t" \
"v_cvt_pk_bf16_f32 v40, v40, v41\n\t" \
"v_cvt_pk_bf16_f32 v41, v42, v43\n\t" \
"v_mfma_f32_16x16x32_bf16 v[48:51], v[76:79], v[120:123], v[48:51]\n\t" \
"ds_write_b64 v157, v[40:41] offset:4096\n\t" \
"v_pk_mul_f32 v[16:17], v[16:17], v[6:7] op_sel_hi:[1,0]\n\t" \
"v_pk_mul_f32 v[18:19], v[18:19], v[6:7] op_sel_hi:[1,0]\n\t" \
"v_cvt_pk_bf16_f32 v16, v16, v17\n\t" \
"v_cvt_pk_bf16_f32 v17, v18, v19\n\t" \
"v_mfma_f32_16x16x32_bf16 v[24:27], v[72:75], v[124:127], v[24:27]\n\t" \
"ds_write_b64 v156, v[16:17] offset:8192\n\t" \
"v_pk_mul_f32 v[44:45], v[44:45], v[6:7] op_sel_hi:[1,0]\n\t" \
"v_pk_mul_f32 v[46:47], v[46:47], v[6:7] op_sel_hi:[1,0]\n\t" \
"v_cvt_pk_bf16_f32 v44, v44, v45\n\t" \
"v_cvt_pk_bf16_f32 v45, v46, v47\n\t" \
"v_mfma_f32_16x16x32_bf16 v[52:55], v[76:79], v[124:127], v[52:55]\n\t" \
"ds_write_b64 v157, v[44:45] offset:8192\n\t" \
"v_pk_mul_f32 v[20:21], v[20:21], v[6:7] op_sel_hi:[1,0]\n\t" \
"v_pk_mul_f32 v[22:23], v[22:23], v[6:7] op_sel_hi:[1,0]\n\t" \
"v_cvt_pk_bf16_f32 v20, v20, v21\n\t" \
"v_cvt_pk_bf16_f32 v21, v22, v23\n\t" \
"v_mfma_f32_16x16x32_bf16 v[28:31], v[72:75], v[128:131], v[28:31]\n\t" \
"ds_write_b64 v156, v[20:21] offset:12288\n\t" \
"v_pk_mul_f32 v[48:49], v[48:49], v[6:7] op_sel_hi:[1,0]\n\t" \
"v_pk_mul_f32 v[50:51], v[50:51], v[6:7] op_sel_hi:[1,0]\n\t" \
"v_cvt_pk_bf16_f32 v48, v48, v49\n\t" \
"v_cvt_pk_bf16_f32 v49, v50, v51\n\t" \
"v_mfma_f32_16x16x32_bf16 v[56:59], v[76:79], v[128:131], v[56:59]\n\t" \
"ds_write_b64 v157, v[48:49] offset:12288\n\t" \
"v_pk_mul_f32 v[24:25], v[24:25], v[6:7] op_sel_hi:[1,0]\n\t" \
"v_pk_mul_f32 v[26:27], v[26:27], v[6:7] op_sel_hi:[1,0]\n\t" \
"v_cvt_pk_bf16_f32 v24, v24, v25\n\t" \
"v_cvt_pk_bf16_f32 v25, v26, v27\n\t" \
"v_mfma_f32_16x16x32_bf16 v[32:35], v[72:75], v[132:135], v[32:35]\n\t" \
"ds_write_b64 v156, v[24:25] offset:16384\n\t" \
"v_pk_mul_f32 v[52:53], v[52:53], v[6:7] op_sel_hi:[1,0]\n\t" \
"v_pk_mul_f32 v[54:55], v[54:55], v[6:7] op_sel_hi:[1,0]\n\t" \
"v_cvt_pk_bf16_f32 v52, v52, v53\n\t" \
"v_cvt_pk_bf16_f32 v53, v54, v55\n\t" \
"v_mfma_f32_16x16x32_bf16 v[60:63], v[76:79], v[132:135], v[60:63]\n\t" \
"ds_write_b64 v157, v[52:53] offset:16384\n\t" \
"v_pk_mul_f32 v[28:29], v[28:29], v[6:7] op_sel_hi:[1,0]\n\t" \
"v_pk_mul_f32 v[30:31], v[30:31], v[6:7] op_sel_hi:[1,0]\n\t" \
"v_cvt_pk_bf16_f32 v28, v28, v29\n\t" \
"v_cvt_pk_bf16_f32 v29, v30, v31\n\t" \
"ds_write_b64 v156, v[28:29] offset:20480\n\t" \
"v_pk_mul_f32 v[56:57], v[56:57], v[6:7] op_sel_hi:[1,0]\n\t" \
"v_pk_mul_f32 v[58:59], v[58:59], v[6:7] op_sel_hi:[1,0]\n\t" \
"v_cvt_pk_bf16_f32 v56, v56, v57\n\t" \
"v_cvt_pk_bf16_f32 v57, v58, v59\n\t" \
"v_pk_mul_f32 v[32:33], v[32:33], v[6:7] op_sel_hi:[1,0]\n\t" \
"v_pk_mul_f32 v[34:35], v[34:35], v[6:7] op_sel_hi:[1,0]\n\t" \
"v_cvt_pk_bf16_f32 v32, v32, v33\n\t" \
"v_cvt_pk_bf16_f32 v33, v34, v35\n\t" \
"v_pk_mul_f32 v[60:61], v[60:61], v[6:7] op_sel_hi:[1,0]\n\t" \
"v_pk_mul_f32 v[62:63], v[62:63], v[6:7] op_sel_hi:[1,0]\n\t" \
"v_cvt_pk_bf16_f32 v60, v60, v61\n\t" \
"v_cvt_pk_bf16_f32 v61, v62, v63\n\t" \
"ds_write_b64 v157, v[56:57] offset:20480\n\t" \
"ds_write_b64 v156, v[32:33] offset:24576\n\t" \
"ds_write_b64 v157, v[60:61] offset:24576\n\t" \
"s_mov_b32 s79, 0x20000\n\t" \
"s_mov_b32 s70, 0\n\t" \
"s_cmp_eq_u32 s74, 0\n\t" \
"s_cbranch_scc0 L_r24_%=\n\t" \
"s_waitcnt vmcnt(0)\n\t" \
"L_r24_%=:\n\t" \
"s_waitcnt vmcnt(24)\n\t" \
"v_add_co_u32 v252, vcc, 1, v252\n\t" \
"s_nop 1\n\t" \
"v_addc_co_u32 v253, vcc, 0, v253, vcc\n\t" \
"s_lshl_b32 s60, s37, 2\n\t" \
"v_readlane_b32 s50, v252, s60\n\t" \
"v_readlane_b32 s51, v253, s60\n\t" \
"s_waitcnt lgkmcnt(0)\n\t" \
"s_barrier\n\t" \
"s_cmp_eq_u32 s74, 0\n\t" \
"s_cbranch_scc0 L_r25_%=\n\t" \
"s_add_u32 s64, s70, s33\n\t" \
"s_and_b32 s60, s64, 15\n\t" \
"s_lshl_b32 s61, s60, 7\n\t" \
"s_add_u32 m0, s72, 0x8000\n\t" \
"s_lshl_b32 s62, s60, 19\n\t" \
"buffer_load_dwordx4 v232, s[4:7], s61 offen offset:0 lds\n\t" \
"buffer_load_dwordx4 v233, s[4:7], s61 offen offset:1024 lds\n\t" \
"buffer_load_dwordx4 v234, s[4:7], s61 offen offset:2048 lds\n\t" \
"buffer_load_dwordx4 v235, s[4:7], s61 offen offset:3072 lds\n\t" \
"s_add_u32 m0, m0, 4096\n\t" \
"s_add_u32 s61, s61, 0x10000\n\t" \
"buffer_load_dwordx4 v232, s[4:7], s61 offen offset:0 lds\n\t" \
"buffer_load_dwordx4 v233, s[4:7], s61 offen offset:1024 lds\n\t" \
"buffer_load_dwordx4 v234, s[4:7], s61 offen offset:2048 lds\n\t" \
"s_add_u32 m0, s73, 0x8000\n\t" \
"s_add_u32 s62, s62, 1024\n\t" \
"buffer_load_dwordx4 v236, s[8:11], s62 offen offset:0 lds\n\t" \
"buffer_load_dwordx4 v237, s[8:11], s62 offen offset:1024 lds\n\t" \
"buffer_load_dwordx4 v238, s[8:11], s62 offen offset:2048 lds\n\t" \
"buffer_load_dwordx4 v239, s[8:11], s62 offen offset:3072 lds\n\t" \
"s_add_u32 m0, m0, 4096\n\t" \
"s_add_u32 s62, s62, 0x20000\n\t" \
"buffer_load_dwordx4 v236, s[8:11], s62 offen offset:0 lds\n\t" \
"buffer_load_dwordx4 v237, s[8:11], s62 offen offset:1024 lds\n\t" \
"buffer_load_dwordx4 v238, s[8:11], s62 offen offset:2048 lds\n\t" \
"buffer_load_dwordx4 v239, s[8:11], s62 offen offset:3072 lds\n\t" \
"L_r25_%=:\n\t" \
"s_barrier\n\t" \
"s_cmp_eq_u32 s74, 1\n\t" \
"s_cbranch_scc0 L_r26_%=\n\t" \
"s_add_u32 s61, s71, 2\n\t" \
"s_lshr_b32 s63, s61, 4\n\t" \
"s_mul_i32 s63, s63, 0x100000\n\t" \
"s_add_u32 s61, s61, s81\n\t" \
"s_and_b32 s61, s61, 15\n\t" \
"s_lshl_b32 s61, s61, 14\n\t" \
"s_add_u32 s61, s61, s63\n\t" \
"s_add_u32 m0, s78, 0x20000\n\t" \
"s_nop 0\n\t" \
"buffer_load_dwordx4 v232, s[12:15], s61 offen offset:0 nt lds\n\t" \
"buffer_load_dwordx4 v233, s[12:15], s61 offen offset:1024 nt lds\n\t" \
"buffer_load_dwordx4 v234, s[12:15], s61 offen offset:2048 nt lds\n\t" \
"buffer_load_dwordx4 v235, s[12:15], s61 offen offset:3072 nt lds\n\t" \
"s_add_u32 m0, m0, 4096\n\t" \
"s_add_u32 s61, s61, 4096\n\t" \
"buffer_load_dwordx4 v232, s[12:15], s61 offen offset:0 nt lds\n\t" \
"buffer_load_dwordx4 v233, s[12:15], s61 offen offset:1024 nt lds\n\t" \
"buffer_load_dwordx4 v234, s[12:15], s61 offen offset:2048 nt lds\n\t" \
"buffer_load_dwordx4 v235, s[12:15], s61 offen offset:3072 nt lds\n\t" \
"s_cmp_eq_u32 s79, 0x10000\n\t" \
"s_cselect_b32 s67, s79, 0xffff8000\n\t" \
"s_add_u32 s79, s79, s67\n\t" \
"s_waitcnt vmcnt(8)\n\t" \
"L_r26_%=:\n\t" \
"s_cmp_eq_u32 s74, 0\n\t" \
"s_cbranch_scc0 L_r27_%=\n\t" \
"s_waitcnt vmcnt(0)\n\t" \
"L_r27_%=:\n\t" \
"s_barrier\n\t" \
"s_mov_b32 s70, 1\n\t" \
"s_cmp_eq_u32 s74, 0\n\t" \
"s_cbranch_scc0 L_r28_%=\n\t" \
"s_add_u32 s64, s70, s33\n\t" \
"s_and_b32 s60, s64, 15\n\t" \
"s_lshl_b32 s61, s60, 7\n\t" \
"s_add_u32 m0, s72, 0x0\n\t" \
"s_lshl_b32 s62, s60, 19\n\t" \
"buffer_load_dwordx4 v232, s[4:7], s61 offen offset:0 lds\n\t" \
"buffer_load_dwordx4 v233, s[4:7], s61 offen offset:1024 lds\n\t" \
"buffer_load_dwordx4 v234, s[4:7], s61 offen offset:2048 lds\n\t" \
"buffer_load_dwordx4 v235, s[4:7], s61 offen offset:3072 lds\n\t" \
"s_add_u32 m0, m0, 4096\n\t" \
"s_add_u32 s61, s61, 0x10000\n\t" \
"buffer_load_dwordx4 v232, s[4:7], s61 offen offset:0 lds\n\t" \
"buffer_load_dwordx4 v233, s[4:7], s61 offen offset:1024 lds\n\t" \
"buffer_load_dwordx4 v234, s[4:7], s61 offen offset:2048 lds\n\t" \
"s_add_u32 m0, s73, 0x0\n\t" \
"s_add_u32 s62, s62, 1024\n\t" \
"buffer_load_dwordx4 v236, s[8:11], s62 offen offset:0 lds\n\t" \
"buffer_load_dwordx4 v237, s[8:11], s62 offen offset:1024 lds\n\t" \
"buffer_load_dwordx4 v238, s[8:11], s62 offen offset:2048 lds\n\t" \
"buffer_load_dwordx4 v239, s[8:11], s62 offen offset:3072 lds\n\t" \
"s_add_u32 m0, m0, 4096\n\t" \
"s_add_u32 s62, s62, 0x20000\n\t" \
"buffer_load_dwordx4 v236, s[8:11], s62 offen offset:0 lds\n\t" \
"buffer_load_dwordx4 v237, s[8:11], s62 offen offset:1024 lds\n\t" \
"buffer_load_dwordx4 v238, s[8:11], s62 offen offset:2048 lds\n\t" \
"buffer_load_dwordx4 v239, s[8:11], s62 offen offset:3072 lds\n\t" \
"L_r28_%=:\n\t" \
"ds_read_b64_tr_b16 v[64:65], v136 offset:32768\n\t" \
"ds_read_b64_tr_b16 v[66:67], v136 offset:33792\n\t" \
"ds_read_b64_tr_b16 v[68:69], v137 offset:32768\n\t" \
"ds_read_b64_tr_b16 v[70:71], v137 offset:33792\n\t" \
"ds_read_b128 v[80:83], v138 offset:32768\n\t" \
"ds_read_b128 v[84:87], v138 offset:34816\n\t" \
"ds_read_b128 v[88:91], v138 offset:36864\n\t" \
"ds_read_b128 v[92:95], v138 offset:38912\n\t" \
"ds_read_b128 v[96:99], v138 offset:40960\n\t" \
"ds_read_b128 v[100:103], v138 offset:43008\n\t" \
"ds_read_b128 v[104:107], v138 offset:45056\n\t" \
"s_waitcnt lgkmcnt(0)\n\t" \
"v_mfma_f32_16x16x32_bf16 v[8:11], v[64:67], v[80:83], 0\n\t" \
"ds_read_b64_tr_b16 v[72:73], v136 offset:40960\n\t" \
"ds_read_b64_tr_b16 v[74:75], v136 offset:41984\n\t" \
"v_mfma_f32_16x16x32_bf16 v[36:39], v[68:71], v[80:83], 0\n\t" \
"ds_read_b64_tr_b16 v[76:77], v137 offset:40960\n\t" \
"ds_read_b64_tr_b16 v[78:79], v137 offset:41984\n\t" \
"v_mfma_f32_16x16x32_bf16 v[12:15], v[64:67], v[84:87], 0\n\t" \
"ds_read_b128 v[108:111], v139 offset:32768\n\t" \
"v_mfma_f32_16x16x32_bf16 v[40:43], v[68:71], v[84:87], 0\n\t" \
"ds_read_b128 v[112:115], v139 offset:34816\n\t" \
"v_mfma_f32_16x16x32_bf16 v[16:19], v[64:67], v[88:91], 0\n\t" \
"ds_read_b128 v[116:119], v139 offset:36864\n\t" \
"v_mfma_f32_16x16x32_bf16 v[44:47], v[68:71], v[88:91], 0\n\t" \
"ds_read_b128 v[120:123], v139 offset:38912\n\t" \
"v_mfma_f32_16x16x32_bf16 v[20:23], v[64:67], v[92:95], 0\n\t" \
"ds_read_b128 v[124:127], v139 offset:40960\n\t" \
"v_mfma_f32_16x16x32_bf16 v[48:51], v[68:71], v[92:95], 0\n\t" \
"ds_read_b128 v[128:131], v139 offset:43008\n\t" \
"v_mfma_f32_16x16x32_bf16 v[24:27], v[64:67], v[96:99], 0\n\t" \
"ds_read_b128 v[132:135], v139 offset:45056\n\t" \
"v_mfma_f32_16x16x32_bf16 v[52:55], v[68:71], v[96:99], 0\n\t" \
"v_mfma_f32_16x16x32_bf16 v[28:31], v[64:67], v[100:103], 0\n\t" \
"v_mfma_f32_16x16x32_bf16 v[56:59], v[68:71], v[100:103], 0\n\t" \
"v_mfma_f32_16x16x32_bf16 v[32:35], v[64:67], v[104:107], 0\n\t" \
"v_mfma_f32_16x16x32_bf16 v[60:63], v[68:71], v[104:107], 0\n\t" \
"s_cmp_eq_u32 s74, 1\n\t" \
"s_cbranch_scc0 L_r29_%=\n\t" \
"s_add_u32 s64, s70, 2\n\t" \
"s_add_u32 s61, s71, s64\n\t" \
"s_lshr_b32 s63, s61, 4\n\t" \
"s_mul_i32 s63, s63, 0x100000\n\t" \
"s_add_u32 s61, s61, s81\n\t" \
"s_and_b32 s61, s61, 15\n\t" \
"s_lshl_b32 s61, s61, 14\n\t" \
"s_add_u32 s61, s61, s63\n\t" \
"s_add_u32 m0, s78, s79\n\t" \
"s_nop 0\n\t" \
"buffer_load_dwordx4 v232, s[12:15], s61 offen offset:0 nt lds\n\t" \
"buffer_load_dwordx4 v233, s[12:15], s61 offen offset:1024 nt lds\n\t" \
"buffer_load_dwordx4 v234, s[12:15], s61 offen offset:2048 nt lds\n\t" \
"buffer_load_dwordx4 v235, s[12:15], s61 offen offset:3072 nt lds\n\t" \
"s_add_u32 m0, m0, 4096\n\t" \
"s_add_u32 s61, s61, 4096\n\t" \
"buffer_load_dwordx4 v232, s[12:15], s61 offen offset:0 nt lds\n\t" \
"buffer_load_dwordx4 v233, s[12:15], s61 offen offset:1024 nt lds\n\t" \
"buffer_load_dwordx4 v234, s[12:15], s61 offen offset:2048 nt lds\n\t" \
"buffer_load_dwordx4 v235, s[12:15], s61 offen offset:3072 nt lds\n\t" \
"s_mov_b32 s61, s64\n\t" \
"s_lshr_b32 s63, s61, 4\n\t" \
"s_mul_i32 s63, s63, 0x100000\n\t" \
"s_add_u32 s61, s61, s81\n\t" \
"s_and_b32 s61, s61, 15\n\t" \
"s_lshl_b32 s61, s61, 14\n\t" \
"s_add_u32 s61, s61, s63\n\t" \
"s_add_u32 m0, s73, s79\n\t" \
"s_nop 0\n\t" \
"buffer_load_dwordx4 v236, s[16:19], s61 offen offset:0 nt lds\n\t" \
"buffer_load_dwordx4 v237, s[16:19], s61 offen offset:1024 nt lds\n\t" \
"buffer_load_dwordx4 v236, s[16:19], s61 offen offset:2048 nt lds\n\t" \
"buffer_load_dwordx4 v237, s[16:19], s61 offen offset:3072 nt lds\n\t" \
"s_add_u32 m0, m0, 4096\n\t" \
"s_add_u32 s61, s61, 4096\n\t" \
"buffer_load_dwordx4 v236, s[16:19], s61 offen offset:0 nt lds\n\t" \
"buffer_load_dwordx4 v237, s[16:19], s61 offen offset:1024 nt lds\n\t" \
"buffer_load_dwordx4 v236, s[16:19], s61 offen offset:2048 nt lds\n\t" \
"buffer_load_dwordx4 v237, s[16:19], s61 offen offset:3072 nt lds\n\t" \
"s_cmp_eq_u32 s79, 0x10000\n\t" \
"s_cselect_b32 s67, s79, 0xffff8000\n\t" \
"s_add_u32 s79, s79, s67\n\t" \
"s_waitcnt vmcnt(16)\n\t" \
"L_r29_%=:\n\t" \
"s_cmp_eq_u32 s74, 0\n\t" \
"s_cbranch_scc0 L_r30_%=\n\t" \
"s_waitcnt vmcnt(0)\n\t" \
"L_r30_%=:\n\t" \
"s_add_u32 s70, s70, 1\n\t" \
"L_p1_%=:\n\t" \
"s_waitcnt lgkmcnt(0)\n\t" \
"s_barrier\n\t" \
"s_cmp_eq_u32 s74, 0\n\t" \
"s_cbranch_scc0 L_r31_%=\n\t" \
"s_add_u32 s64, s70, s33\n\t" \
"s_and_b32 s60, s64, 15\n\t" \
"s_lshl_b32 s61, s60, 7\n\t" \
"s_add_u32 m0, s72, 0x8000\n\t" \
"s_lshl_b32 s62, s60, 19\n\t" \
"buffer_load_dwordx4 v232, s[4:7], s61 offen offset:0 lds\n\t" \
"buffer_load_dwordx4 v233, s[4:7], s61 offen offset:1024 lds\n\t" \
"buffer_load_dwordx4 v234, s[4:7], s61 offen offset:2048 lds\n\t" \
"buffer_load_dwordx4 v235, s[4:7], s61 offen offset:3072 lds\n\t" \
"s_add_u32 m0, m0, 4096\n\t" \
"s_add_u32 s61, s61, 0x10000\n\t" \
"buffer_load_dwordx4 v232, s[4:7], s61 offen offset:0 lds\n\t" \
"buffer_load_dwordx4 v233, s[4:7], s61 offen offset:1024 lds\n\t" \
"buffer_load_dwordx4 v234, s[4:7], s61 offen offset:2048 lds\n\t" \
"s_add_u32 m0, s73, 0x8000\n\t" \
"s_add_u32 s62, s62, 1024\n\t" \
"buffer_load_dwordx4 v236, s[8:11], s62 offen offset:0 lds\n\t" \
"buffer_load_dwordx4 v237, s[8:11], s62 offen offset:1024 lds\n\t" \
"buffer_load_dwordx4 v238, s[8:11], s62 offen offset:2048 lds\n\t" \
"buffer_load_dwordx4 v239, s[8:11], s62 offen offset:3072 lds\n\t" \
"s_add_u32 m0, m0, 4096\n\t" \
"s_add_u32 s62, s62, 0x20000\n\t" \
"buffer_load_dwordx4 v236, s[8:11], s62 offen offset:0 lds\n\t" \
"buffer_load_dwordx4 v237, s[8:11], s62 offen offset:1024 lds\n\t" \
"buffer_load_dwordx4 v238, s[8:11], s62 offen offset:2048 lds\n\t" \
"buffer_load_dwordx4 v239, s[8:11], s62 offen offset:3072 lds\n\t" \
"L_r31_%=:\n\t" \
"v_mfma_f32_16x16x32_bf16 v[8:11], v[72:75], v[108:111], v[8:11]\n\t" \
"ds_read_b64_tr_b16 v[64:65], v136 offset:0\n\t" \
"ds_read_b64_tr_b16 v[66:67], v136 offset:1024\n\t" \
"v_mfma_f32_16x16x32_bf16 v[36:39], v[76:79], v[108:111], v[36:39]\n\t" \
"ds_read_b64_tr_b16 v[68:69], v137 offset:0\n\t" \
"ds_read_b64_tr_b16 v[70:71], v137 offset:1024\n\t" \
"v_mfma_f32_16x16x32_bf16 v[12:15], v[72:75], v[112:115], v[12:15]\n\t" \
"ds_read_b128 v[80:83], v138 offset:0\n\t" \
"v_mfma_f32_16x16x32_bf16 v[40:43], v[76:79], v[112:115], v[40:43]\n\t" \
"ds_read_b128 v[84:87], v138 offset:2048\n\t" \
"v_mfma_f32_16x16x32_bf16 v[16:19], v[72:75], v[116:119], v[16:19]\n\t" \
"ds_read_b128 v[88:91], v138 offset:4096\n\t" \
"v_mfma_f32_16x16x32_bf16 v[44:47], v[76:79], v[116:119], v[44:47]\n\t" \
"ds_read_b128 v[92:95], v138 offset:6144\n\t" \
"v_mfma_f32_16x16x32_bf16 v[20:23], v[72:75], v[120:123], v[20:23]\n\t" \
"ds_read_b128 v[96:99], v138 offset:8192\n\t" \
"v_mfma_f32_16x16x32_bf16 v[48:51], v[76:79], v[120:123], v[48:51]\n\t" \
"ds_read_b128 v[100:103], v138 offset:10240\n\t" \
"v_mfma_f32_16x16x32_bf16 v[24:27], v[72:75], v[124:127], v[24:27]\n\t" \
"ds_read_b128 v[104:107], v138 offset:12288\n\t" \
"v_mfma_f32_16x16x32_bf16 v[52:55], v[76:79], v[124:127], v[52:55]\n\t" \
"v_mfma_f32_16x16x32_bf16 v[28:31], v[72:75], v[128:131], v[28:31]\n\t" \
"v_mfma_f32_16x16x32_bf16 v[56:59], v[76:79], v[128:131], v[56:59]\n\t" \
"v_mfma_f32_16x16x32_bf16 v[32:35], v[72:75], v[132:135], v[32:35]\n\t" \
"v_mfma_f32_16x16x32_bf16 v[60:63], v[76:79], v[132:135], v[60:63]\n\t" \
"s_waitcnt lgkmcnt(0)\n\t" \
"v_mfma_f32_16x16x32_bf16 v[8:11], v[64:67], v[80:83], v[8:11]\n\t" \
"ds_read_b64_tr_b16 v[72:73], v136 offset:8192\n\t" \
"ds_read_b64_tr_b16 v[74:75], v136 offset:9216\n\t" \
"v_mfma_f32_16x16x32_bf16 v[36:39], v[68:71], v[80:83], v[36:39]\n\t" \
"ds_read_b64_tr_b16 v[76:77], v137 offset:8192\n\t" \
"ds_read_b64_tr_b16 v[78:79], v137 offset:9216\n\t" \
"v_mfma_f32_16x16x32_bf16 v[12:15], v[64:67], v[84:87], v[12:15]\n\t" \
"ds_read_b128 v[108:111], v139 offset:0\n\t" \
"v_mfma_f32_16x16x32_bf16 v[40:43], v[68:71], v[84:87], v[40:43]\n\t" \
"ds_read_b128 v[112:115], v139 offset:2048\n\t" \
"v_mfma_f32_16x16x32_bf16 v[16:19], v[64:67], v[88:91], v[16:19]\n\t" \
"ds_read_b128 v[116:119], v139 offset:4096\n\t" \
"v_mfma_f32_16x16x32_bf16 v[44:47], v[68:71], v[88:91], v[44:47]\n\t" \
"ds_read_b128 v[120:123], v139 offset:6144\n\t" \
"v_mfma_f32_16x16x32_bf16 v[20:23], v[64:67], v[92:95], v[20:23]\n\t" \
"ds_read_b128 v[124:127], v139 offset:8192\n\t" \
"v_mfma_f32_16x16x32_bf16 v[48:51], v[68:71], v[92:95], v[48:51]\n\t" \
"ds_read_b128 v[128:131], v139 offset:10240\n\t" \
"v_mfma_f32_16x16x32_bf16 v[24:27], v[64:67], v[96:99], v[24:27]\n\t" \
"ds_read_b128 v[132:135], v139 offset:12288\n\t" \
"v_mfma_f32_16x16x32_bf16 v[52:55], v[68:71], v[96:99], v[52:55]\n\t" \
"v_mfma_f32_16x16x32_bf16 v[28:31], v[64:67], v[100:103], v[28:31]\n\t" \
"v_mfma_f32_16x16x32_bf16 v[56:59], v[68:71], v[100:103], v[56:59]\n\t" \
"v_mfma_f32_16x16x32_bf16 v[32:35], v[64:67], v[104:107], v[32:35]\n\t" \
"v_mfma_f32_16x16x32_bf16 v[60:63], v[68:71], v[104:107], v[60:63]\n\t" \
"s_cmp_eq_u32 s74, 1\n\t" \
"s_cbranch_scc0 L_r32_%=\n\t" \
"s_add_u32 s64, s70, 2\n\t" \
"s_add_u32 s61, s71, s64\n\t" \
"s_lshr_b32 s63, s61, 4\n\t" \
"s_mul_i32 s63, s63, 0x100000\n\t" \
"s_add_u32 s61, s61, s81\n\t" \
"s_and_b32 s61, s61, 15\n\t" \
"s_lshl_b32 s61, s61, 14\n\t" \
"s_add_u32 s61, s61, s63\n\t" \
"s_add_u32 m0, s78, s79\n\t" \
"s_nop 0\n\t" \
"buffer_load_dwordx4 v232, s[12:15], s61 offen offset:0 nt lds\n\t" \
"buffer_load_dwordx4 v233, s[12:15], s61 offen offset:1024 nt lds\n\t" \
"buffer_load_dwordx4 v234, s[12:15], s61 offen offset:2048 nt lds\n\t" \
"buffer_load_dwordx4 v235, s[12:15], s61 offen offset:3072 nt lds\n\t" \
"s_add_u32 m0, m0, 4096\n\t" \
"s_add_u32 s61, s61, 4096\n\t" \
"buffer_load_dwordx4 v232, s[12:15], s61 offen offset:0 nt lds\n\t" \
"buffer_load_dwordx4 v233, s[12:15], s61 offen offset:1024 nt lds\n\t" \
"buffer_load_dwordx4 v234, s[12:15], s61 offen offset:2048 nt lds\n\t" \
"buffer_load_dwordx4 v235, s[12:15], s61 offen offset:3072 nt lds\n\t" \
"s_mov_b32 s61, s64\n\t" \
"s_lshr_b32 s63, s61, 4\n\t" \
"s_mul_i32 s63, s63, 0x100000\n\t" \
"s_add_u32 s61, s61, s81\n\t" \
"s_and_b32 s61, s61, 15\n\t" \
"s_lshl_b32 s61, s61, 14\n\t" \
"s_add_u32 s61, s61, s63\n\t" \
"s_add_u32 m0, s73, s79\n\t" \
"s_nop 0\n\t" \
"buffer_load_dwordx4 v236, s[16:19], s61 offen offset:0 nt lds\n\t" \
"buffer_load_dwordx4 v237, s[16:19], s61 offen offset:1024 nt lds\n\t" \
"buffer_load_dwordx4 v236, s[16:19], s61 offen offset:2048 nt lds\n\t" \
"buffer_load_dwordx4 v237, s[16:19], s61 offen offset:3072 nt lds\n\t" \
"s_add_u32 m0, m0, 4096\n\t" \
"s_add_u32 s61, s61, 4096\n\t" \
"buffer_load_dwordx4 v236, s[16:19], s61 offen offset:0 nt lds\n\t" \
"buffer_load_dwordx4 v237, s[16:19], s61 offen offset:1024 nt lds\n\t" \
"buffer_load_dwordx4 v236, s[16:19], s61 offen offset:2048 nt lds\n\t" \
"buffer_load_dwordx4 v237, s[16:19], s61 offen offset:3072 nt lds\n\t" \
"s_cmp_eq_u32 s79, 0x10000\n\t" \
"s_cselect_b32 s67, s79, 0xffff8000\n\t" \
"s_add_u32 s79, s79, s67\n\t" \
"s_waitcnt vmcnt(16)\n\t" \
"L_r32_%=:\n\t" \
"s_cmp_eq_u32 s74, 0\n\t" \
"s_cbranch_scc0 L_r33_%=\n\t" \
"s_waitcnt vmcnt(0)\n\t" \
"L_r33_%=:\n\t" \
"s_add_u32 s70, s70, 1\n\t" \
"s_waitcnt lgkmcnt(0)\n\t" \
"s_barrier\n\t" \
"s_cmp_eq_u32 s74, 0\n\t" \
"s_cbranch_scc0 L_r34_%=\n\t" \
"s_add_u32 s64, s70, s33\n\t" \
"s_and_b32 s60, s64, 15\n\t" \
"s_lshl_b32 s61, s60, 7\n\t" \
"s_add_u32 m0, s72, 0x0\n\t" \
"s_lshl_b32 s62, s60, 19\n\t" \
"buffer_load_dwordx4 v232, s[4:7], s61 offen offset:0 lds\n\t" \
"buffer_load_dwordx4 v233, s[4:7], s61 offen offset:1024 lds\n\t" \
"buffer_load_dwordx4 v234, s[4:7], s61 offen offset:2048 lds\n\t" \
"buffer_load_dwordx4 v235, s[4:7], s61 offen offset:3072 lds\n\t" \
"s_add_u32 m0, m0, 4096\n\t" \
"s_add_u32 s61, s61, 0x10000\n\t" \
"buffer_load_dwordx4 v232, s[4:7], s61 offen offset:0 lds\n\t" \
"buffer_load_dwordx4 v233, s[4:7], s61 offen offset:1024 lds\n\t" \
"buffer_load_dwordx4 v234, s[4:7], s61 offen offset:2048 lds\n\t" \
"s_add_u32 m0, s73, 0x0\n\t" \
"s_add_u32 s62, s62, 1024\n\t" \
"buffer_load_dwordx4 v236, s[8:11], s62 offen offset:0 lds\n\t" \
"buffer_load_dwordx4 v237, s[8:11], s62 offen offset:1024 lds\n\t" \
"buffer_load_dwordx4 v238, s[8:11], s62 offen offset:2048 lds\n\t" \
"buffer_load_dwordx4 v239, s[8:11], s62 offen offset:3072 lds\n\t" \
"s_add_u32 m0, m0, 4096\n\t" \
"s_add_u32 s62, s62, 0x20000\n\t" \
"buffer_load_dwordx4 v236, s[8:11], s62 offen offset:0 lds\n\t" \
"buffer_load_dwordx4 v237, s[8:11], s62 offen offset:1024 lds\n\t" \
"buffer_load_dwordx4 v238, s[8:11], s62 offen offset:2048 lds\n\t" \
"buffer_load_dwordx4 v239, s[8:11], s62 offen offset:3072 lds\n\t" \
"L_r34_%=:\n\t" \
"v_mfma_f32_16x16x32_bf16 v[8:11], v[72:75], v[108:111], v[8:11]\n\t" \
"ds_read_b64_tr_b16 v[64:65], v136 offset:32768\n\t" \
"ds_read_b64_tr_b16 v[66:67], v136 offset:33792\n\t" \
"v_mfma_f32_16x16x32_bf16 v[36:39], v[76:79], v[108:111], v[36:39]\n\t" \
"ds_read_b64_tr_b16 v[68:69], v137 offset:32768\n\t" \
"ds_read_b64_tr_b16 v[70:71], v137 offset:33792\n\t" \
"v_mfma_f32_16x16x32_bf16 v[12:15], v[72:75], v[112:115], v[12:15]\n\t" \
"ds_read_b128 v[80:83], v138 offset:32768\n\t" \
"v_mfma_f32_16x16x32_bf16 v[40:43], v[76:79], v[112:115], v[40:43]\n\t" \
"ds_read_b128 v[84:87], v138 offset:34816\n\t" \
"v_mfma_f32_16x16x32_bf16 v[16:19], v[72:75], v[116:119], v[16:19]\n\t" \
"ds_read_b128 v[88:91], v138 offset:36864\n\t" \
"v_mfma_f32_16x16x32_bf16 v[44:47], v[76:79], v[116:119], v[44:47]\n\t" \
"ds_read_b128 v[92:95], v138 offset:38912\n\t" \
"v_mfma_f32_16x16x32_bf16 v[20:23], v[72:75], v[120:123], v[20:23]\n\t" \
"ds_read_b128 v[96:99], v138 offset:40960\n\t" \
"v_mfma_f32_16x16x32_bf16 v[48:51], v[76:79], v[120:123], v[48:51]\n\t" \
"ds_read_b128 v[100:103], v138 offset:43008\n\t" \
"v_mfma_f32_16x16x32_bf16 v[24:27], v[72:75], v[124:127], v[24:27]\n\t" \
"ds_read_b128 v[104:107], v138 offset:45056\n\t" \
"v_mfma_f32_16x16x32_bf16 v[52:55], v[76:79], v[124:127], v[52:55]\n\t" \
"v_mfma_f32_16x16x32_bf16 v[28:31], v[72:75], v[128:131], v[28:31]\n\t" \
"v_mfma_f32_16x16x32_bf16 v[56:59], v[76:79], v[128:131], v[56:59]\n\t" \
"v_mfma_f32_16x16x32_bf16 v[32:35], v[72:75], v[132:135], v[32:35]\n\t" \
"v_mfma_f32_16x16x32_bf16 v[60:63], v[76:79], v[132:135], v[60:63]\n\t" \
"s_waitcnt lgkmcnt(0)\n\t" \
"v_mfma_f32_16x16x32_bf16 v[8:11], v[64:67], v[80:83], v[8:11]\n\t" \
"ds_read_b64_tr_b16 v[72:73], v136 offset:40960\n\t" \
"ds_read_b64_tr_b16 v[74:75], v136 offset:41984\n\t" \
"v_mfma_f32_16x16x32_bf16 v[36:39], v[68:71], v[80:83], v[36:39]\n\t" \
"ds_read_b64_tr_b16 v[76:77], v137 offset:40960\n\t" \
"ds_read_b64_tr_b16 v[78:79], v137 offset:41984\n\t" \
"v_mfma_f32_16x16x32_bf16 v[12:15], v[64:67], v[84:87], v[12:15]\n\t" \
"ds_read_b128 v[108:111], v139 offset:32768\n\t" \
"v_mfma_f32_16x16x32_bf16 v[40:43], v[68:71], v[84:87], v[40:43]\n\t" \
"ds_read_b128 v[112:115], v139 offset:34816\n\t" \
"v_mfma_f32_16x16x32_bf16 v[16:19], v[64:67], v[88:91], v[16:19]\n\t" \
"ds_read_b128 v[116:119], v139 offset:36864\n\t" \
"v_mfma_f32_16x16x32_bf16 v[44:47], v[68:71], v[88:91], v[44:47]\n\t" \
"ds_read_b128 v[120:123], v139 offset:38912\n\t" \
"v_mfma_f32_16x16x32_bf16 v[20:23], v[64:67], v[92:95], v[20:23]\n\t" \
"ds_read_b128 v[124:127], v139 offset:40960\n\t" \
"v_mfma_f32_16x16x32_bf16 v[48:51], v[68:71], v[92:95], v[48:51]\n\t" \
"ds_read_b128 v[128:131], v139 offset:43008\n\t" \
"v_mfma_f32_16x16x32_bf16 v[24:27], v[64:67], v[96:99], v[24:27]\n\t" \
"ds_read_b128 v[132:135], v139 offset:45056\n\t" \
"v_mfma_f32_16x16x32_bf16 v[52:55], v[68:71], v[96:99], v[52:55]\n\t" \
"v_mfma_f32_16x16x32_bf16 v[28:31], v[64:67], v[100:103], v[28:31]\n\t" \
"v_mfma_f32_16x16x32_bf16 v[56:59], v[68:71], v[100:103], v[56:59]\n\t" \
"v_mfma_f32_16x16x32_bf16 v[32:35], v[64:67], v[104:107], v[32:35]\n\t" \
"v_mfma_f32_16x16x32_bf16 v[60:63], v[68:71], v[104:107], v[60:63]\n\t" \
"s_cmp_eq_u32 s74, 1\n\t" \
"s_cbranch_scc0 L_r35_%=\n\t" \
"s_add_u32 s64, s70, 2\n\t" \
"s_add_u32 s61, s71, s64\n\t" \
"s_lshr_b32 s63, s61, 4\n\t" \
"s_mul_i32 s63, s63, 0x100000\n\t" \
"s_add_u32 s61, s61, s81\n\t" \
"s_and_b32 s61, s61, 15\n\t" \
"s_lshl_b32 s61, s61, 14\n\t" \
"s_add_u32 s61, s61, s63\n\t" \
"s_add_u32 m0, s78, s79\n\t" \
"s_nop 0\n\t" \
"buffer_load_dwordx4 v232, s[12:15], s61 offen offset:0 nt lds\n\t" \
"buffer_load_dwordx4 v233, s[12:15], s61 offen offset:1024 nt lds\n\t" \
"buffer_load_dwordx4 v234, s[12:15], s61 offen offset:2048 nt lds\n\t" \
"buffer_load_dwordx4 v235, s[12:15], s61 offen offset:3072 nt lds\n\t" \
"s_add_u32 m0, m0, 4096\n\t" \
"s_add_u32 s61, s61, 4096\n\t" \
"buffer_load_dwordx4 v232, s[12:15], s61 offen offset:0 nt lds\n\t" \
"buffer_load_dwordx4 v233, s[12:15], s61 offen offset:1024 nt lds\n\t" \
"buffer_load_dwordx4 v234, s[12:15], s61 offen offset:2048 nt lds\n\t" \
"buffer_load_dwordx4 v235, s[12:15], s61 offen offset:3072 nt lds\n\t" \
"s_mov_b32 s61, s64\n\t" \
"s_lshr_b32 s63, s61, 4\n\t" \
"s_mul_i32 s63, s63, 0x100000\n\t" \
"s_add_u32 s61, s61, s81\n\t" \
"s_and_b32 s61, s61, 15\n\t" \
"s_lshl_b32 s61, s61, 14\n\t" \
"s_add_u32 s61, s61, s63\n\t" \
"s_add_u32 m0, s73, s79\n\t" \
"s_nop 0\n\t" \
"buffer_load_dwordx4 v236, s[16:19], s61 offen offset:0 nt lds\n\t" \
"buffer_load_dwordx4 v237, s[16:19], s61 offen offset:1024 nt lds\n\t" \
"buffer_load_dwordx4 v236, s[16:19], s61 offen offset:2048 nt lds\n\t" \
"buffer_load_dwordx4 v237, s[16:19], s61 offen offset:3072 nt lds\n\t" \
"s_add_u32 m0, m0, 4096\n\t" \
"s_add_u32 s61, s61, 4096\n\t" \
"buffer_load_dwordx4 v236, s[16:19], s61 offen offset:0 nt lds\n\t" \
"buffer_load_dwordx4 v237, s[16:19], s61 offen offset:1024 nt lds\n\t" \
"buffer_load_dwordx4 v236, s[16:19], s61 offen offset:2048 nt lds\n\t" \
"buffer_load_dwordx4 v237, s[16:19], s61 offen offset:3072 nt lds\n\t" \
"s_cmp_eq_u32 s79, 0x10000\n\t" \
"s_cselect_b32 s67, s79, 0xffff8000\n\t" \
"s_add_u32 s79, s79, s67\n\t" \
"s_waitcnt vmcnt(16)\n\t" \
"L_r35_%=:\n\t" \
"s_cmp_eq_u32 s74, 0\n\t" \
"s_cbranch_scc0 L_r36_%=\n\t" \
"s_waitcnt vmcnt(0)\n\t" \
"L_r36_%=:\n\t" \
"s_add_u32 s70, s70, 1\n\t" \
"s_cmp_lt_u32 s70, 16\n\t" \
"s_cbranch_scc1 L_p1_%=\n\t" \
"s_waitcnt lgkmcnt(0)\n\t" \
"s_barrier\n\t" \
"v_mfma_f32_16x16x32_bf16 v[8:11], v[72:75], v[108:111], v[8:11]\n\t" \
"ds_read_b64_tr_b16 v[64:65], v136 offset:0\n\t" \
"ds_read_b64_tr_b16 v[66:67], v136 offset:1024\n\t" \
"v_mfma_f32_16x16x32_bf16 v[36:39], v[76:79], v[108:111], v[36:39]\n\t" \
"ds_read_b64_tr_b16 v[68:69], v137 offset:0\n\t" \
"ds_read_b64_tr_b16 v[70:71], v137 offset:1024\n\t" \
"v_mfma_f32_16x16x32_bf16 v[12:15], v[72:75], v[112:115], v[12:15]\n\t" \
"ds_read_b128 v[80:83], v138 offset:0\n\t" \
"v_mfma_f32_16x16x32_bf16 v[40:43], v[76:79], v[112:115], v[40:43]\n\t" \
"ds_read_b128 v[84:87], v138 offset:2048\n\t" \
"v_mfma_f32_16x16x32_bf16 v[16:19], v[72:75], v[116:119], v[16:19]\n\t" \
"ds_read_b128 v[88:91], v138 offset:4096\n\t" \
"v_mfma_f32_16x16x32_bf16 v[44:47], v[76:79], v[116:119], v[44:47]\n\t" \
"ds_read_b128 v[92:95], v138 offset:6144\n\t" \
"v_mfma_f32_16x16x32_bf16 v[20:23], v[72:75], v[120:123], v[20:23]\n\t" \
"ds_read_b128 v[96:99], v138 offset:8192\n\t" \
"v_mfma_f32_16x16x32_bf16 v[48:51], v[76:79], v[120:123], v[48:51]\n\t" \
"ds_read_b128 v[100:103], v138 offset:10240\n\t" \
"v_mfma_f32_16x16x32_bf16 v[24:27], v[72:75], v[124:127], v[24:27]\n\t" \
"ds_read_b128 v[104:107], v138 offset:12288\n\t" \
"v_mfma_f32_16x16x32_bf16 v[52:55], v[76:79], v[124:127], v[52:55]\n\t" \
"v_mfma_f32_16x16x32_bf16 v[28:31], v[72:75], v[128:131], v[28:31]\n\t" \
"v_mfma_f32_16x16x32_bf16 v[56:59], v[76:79], v[128:131], v[56:59]\n\t" \
"v_mfma_f32_16x16x32_bf16 v[32:35], v[72:75], v[132:135], v[32:35]\n\t" \
"v_mfma_f32_16x16x32_bf16 v[60:63], v[76:79], v[132:135], v[60:63]\n\t" \
"s_waitcnt lgkmcnt(0)\n\t" \
"v_mfma_f32_16x16x32_bf16 v[8:11], v[64:67], v[80:83], v[8:11]\n\t" \
"ds_read_b64_tr_b16 v[72:73], v136 offset:8192\n\t" \
"ds_read_b64_tr_b16 v[74:75], v136 offset:9216\n\t" \
"v_mfma_f32_16x16x32_bf16 v[36:39], v[68:71], v[80:83], v[36:39]\n\t" \
"ds_read_b64_tr_b16 v[76:77], v137 offset:8192\n\t" \
"ds_read_b64_tr_b16 v[78:79], v137 offset:9216\n\t" \
"v_mfma_f32_16x16x32_bf16 v[12:15], v[64:67], v[84:87], v[12:15]\n\t" \
"ds_read_b128 v[108:111], v139 offset:0\n\t" \
"v_mfma_f32_16x16x32_bf16 v[40:43], v[68:71], v[84:87], v[40:43]\n\t" \
"ds_read_b128 v[112:115], v139 offset:2048\n\t" \
"v_mfma_f32_16x16x32_bf16 v[16:19], v[64:67], v[88:91], v[16:19]\n\t" \
"ds_read_b128 v[116:119], v139 offset:4096\n\t" \
"v_mfma_f32_16x16x32_bf16 v[44:47], v[68:71], v[88:91], v[44:47]\n\t" \
"ds_read_b128 v[120:123], v139 offset:6144\n\t" \
"v_mfma_f32_16x16x32_bf16 v[20:23], v[64:67], v[92:95], v[20:23]\n\t" \
"ds_read_b128 v[124:127], v139 offset:8192\n\t" \
"v_mfma_f32_16x16x32_bf16 v[48:51], v[68:71], v[92:95], v[48:51]\n\t" \
"ds_read_b128 v[128:131], v139 offset:10240\n\t" \
"v_mfma_f32_16x16x32_bf16 v[24:27], v[64:67], v[96:99], v[24:27]\n\t" \
"ds_read_b128 v[132:135], v139 offset:12288\n\t" \
"v_mfma_f32_16x16x32_bf16 v[52:55], v[68:71], v[96:99], v[52:55]\n\t" \
"v_mfma_f32_16x16x32_bf16 v[28:31], v[64:67], v[100:103], v[28:31]\n\t" \
"v_mfma_f32_16x16x32_bf16 v[56:59], v[68:71], v[100:103], v[56:59]\n\t" \
"v_mfma_f32_16x16x32_bf16 v[32:35], v[64:67], v[104:107], v[32:35]\n\t" \
"v_mfma_f32_16x16x32_bf16 v[60:63], v[68:71], v[104:107], v[60:63]\n\t" \
"s_waitcnt lgkmcnt(0)\n\t" \
"v_mov_b32 v6, s80\n\t" \
"v_mfma_f32_16x16x32_bf16 v[8:11], v[72:75], v[108:111], v[8:11]\n\t" \
"v_mfma_f32_16x16x32_bf16 v[36:39], v[76:79], v[108:111], v[36:39]\n\t" \
"v_mfma_f32_16x16x32_bf16 v[12:15], v[72:75], v[112:115], v[12:15]\n\t" \
"v_mfma_f32_16x16x32_bf16 v[40:43], v[76:79], v[112:115], v[40:43]\n\t" \
"s_nop 4\n\t" \
"v_pk_mul_f32 v[8:9], v[8:9], v[6:7] op_sel_hi:[1,0]\n\t" \
"v_pk_mul_f32 v[10:11], v[10:11], v[6:7] op_sel_hi:[1,0]\n\t" \
"v_cvt_pk_bf16_f32 v8, v8, v9\n\t" \
"v_cvt_pk_bf16_f32 v9, v10, v11\n\t" \
"v_mfma_f32_16x16x32_bf16 v[16:19], v[72:75], v[116:119], v[16:19]\n\t" \
"ds_write_b64 v156, v[8:9] offset:32768\n\t" \
"v_pk_mul_f32 v[36:37], v[36:37], v[6:7] op_sel_hi:[1,0]\n\t" \
"v_pk_mul_f32 v[38:39], v[38:39], v[6:7] op_sel_hi:[1,0]\n\t" \
"v_cvt_pk_bf16_f32 v36, v36, v37\n\t" \
"v_cvt_pk_bf16_f32 v37, v38, v39\n\t" \
"v_mfma_f32_16x16x32_bf16 v[44:47], v[76:79], v[116:119], v[44:47]\n\t" \
"ds_write_b64 v157, v[36:37] offset:32768\n\t" \
"v_pk_mul_f32 v[12:13], v[12:13], v[6:7] op_sel_hi:[1,0]\n\t" \
"v_pk_mul_f32 v[14:15], v[14:15], v[6:7] op_sel_hi:[1,0]\n\t" \
"v_cvt_pk_bf16_f32 v12, v12, v13\n\t" \
"v_cvt_pk_bf16_f32 v13, v14, v15\n\t" \
"v_mfma_f32_16x16x32_bf16 v[20:23], v[72:75], v[120:123], v[20:23]\n\t" \
"ds_write_b64 v156, v[12:13] offset:36864\n\t" \
"v_pk_mul_f32 v[40:41], v[40:41], v[6:7] op_sel_hi:[1,0]\n\t" \
"v_pk_mul_f32 v[42:43], v[42:43], v[6:7] op_sel_hi:[1,0]\n\t" \
"v_cvt_pk_bf16_f32 v40, v40, v41\n\t" \
"v_cvt_pk_bf16_f32 v41, v42, v43\n\t" \
"v_mfma_f32_16x16x32_bf16 v[48:51], v[76:79], v[120:123], v[48:51]\n\t" \
"ds_write_b64 v157, v[40:41] offset:36864\n\t" \
"v_pk_mul_f32 v[16:17], v[16:17], v[6:7] op_sel_hi:[1,0]\n\t" \
"v_pk_mul_f32 v[18:19], v[18:19], v[6:7] op_sel_hi:[1,0]\n\t" \
"v_cvt_pk_bf16_f32 v16, v16, v17\n\t" \
"v_cvt_pk_bf16_f32 v17, v18, v19\n\t" \
"v_mfma_f32_16x16x32_bf16 v[24:27], v[72:75], v[124:127], v[24:27]\n\t" \
"ds_write_b64 v156, v[16:17] offset:40960\n\t" \
"v_pk_mul_f32 v[44:45], v[44:45], v[6:7] op_sel_hi:[1,0]\n\t" \
"v_pk_mul_f32 v[46:47], v[46:47], v[6:7] op_sel_hi:[1,0]\n\t" \
"v_cvt_pk_bf16_f32 v44, v44, v45\n\t" \
"v_cvt_pk_bf16_f32 v45, v46, v47\n\t" \
"v_mfma_f32_16x16x32_bf16 v[52:55], v[76:79], v[124:127], v[52:55]\n\t" \
"ds_write_b64 v157, v[44:45] offset:40960\n\t" \
"v_pk_mul_f32 v[20:21], v[20:21], v[6:7] op_sel_hi:[1,0]\n\t" \
"v_pk_mul_f32 v[22:23], v[22:23], v[6:7] op_sel_hi:[1,0]\n\t" \
"v_cvt_pk_bf16_f32 v20, v20, v21\n\t" \
"v_cvt_pk_bf16_f32 v21, v22, v23\n\t" \
"v_mfma_f32_16x16x32_bf16 v[28:31], v[72:75], v[128:131], v[28:31]\n\t" \
"ds_write_b64 v156, v[20:21] offset:45056\n\t" \
"v_pk_mul_f32 v[48:49], v[48:49], v[6:7] op_sel_hi:[1,0]\n\t" \
"v_pk_mul_f32 v[50:51], v[50:51], v[6:7] op_sel_hi:[1,0]\n\t" \
"v_cvt_pk_bf16_f32 v48, v48, v49\n\t" \
"v_cvt_pk_bf16_f32 v49, v50, v51\n\t" \
"v_mfma_f32_16x16x32_bf16 v[56:59], v[76:79], v[128:131], v[56:59]\n\t" \
"ds_write_b64 v157, v[48:49] offset:45056\n\t" \
"v_pk_mul_f32 v[24:25], v[24:25], v[6:7] op_sel_hi:[1,0]\n\t" \
"v_pk_mul_f32 v[26:27], v[26:27], v[6:7] op_sel_hi:[1,0]\n\t" \
"v_cvt_pk_bf16_f32 v24, v24, v25\n\t" \
"v_cvt_pk_bf16_f32 v25, v26, v27\n\t" \
"v_mfma_f32_16x16x32_bf16 v[32:35], v[72:75], v[132:135], v[32:35]\n\t" \
"ds_write_b64 v156, v[24:25] offset:49152\n\t" \
"v_pk_mul_f32 v[52:53], v[52:53], v[6:7] op_sel_hi:[1,0]\n\t" \
"v_pk_mul_f32 v[54:55], v[54:55], v[6:7] op_sel_hi:[1,0]\n\t" \
"v_cvt_pk_bf16_f32 v52, v52, v53\n\t" \
"v_cvt_pk_bf16_f32 v53, v54, v55\n\t" \
"v_mfma_f32_16x16x32_bf16 v[60:63], v[76:79], v[132:135], v[60:63]\n\t" \
"ds_write_b64 v157, v[52:53] offset:49152\n\t" \
"v_pk_mul_f32 v[28:29], v[28:29], v[6:7] op_sel_hi:[1,0]\n\t" \
"v_pk_mul_f32 v[30:31], v[30:31], v[6:7] op_sel_hi:[1,0]\n\t" \
"v_cvt_pk_bf16_f32 v28, v28, v29\n\t" \
"v_cvt_pk_bf16_f32 v29, v30, v31\n\t" \
"ds_write_b64 v156, v[28:29] offset:53248\n\t" \
"v_pk_mul_f32 v[56:57], v[56:57], v[6:7] op_sel_hi:[1,0]\n\t" \
"v_pk_mul_f32 v[58:59], v[58:59], v[6:7] op_sel_hi:[1,0]\n\t" \
"v_cvt_pk_bf16_f32 v56, v56, v57\n\t" \
"v_cvt_pk_bf16_f32 v57, v58, v59\n\t" \
"v_pk_mul_f32 v[32:33], v[32:33], v[6:7] op_sel_hi:[1,0]\n\t" \
"v_pk_mul_f32 v[34:35], v[34:35], v[6:7] op_sel_hi:[1,0]\n\t" \
"v_cvt_pk_bf16_f32 v32, v32, v33\n\t" \
"v_cvt_pk_bf16_f32 v33, v34, v35\n\t" \
"v_pk_mul_f32 v[60:61], v[60:61], v[6:7] op_sel_hi:[1,0]\n\t" \
"v_pk_mul_f32 v[62:63], v[62:63], v[6:7] op_sel_hi:[1,0]\n\t" \
"v_cvt_pk_bf16_f32 v60, v60, v61\n\t" \
"v_cvt_pk_bf16_f32 v61, v62, v63\n\t" \
"ds_write_b64 v157, v[56:57] offset:53248\n\t" \
"ds_write_b64 v156, v[32:33] offset:57344\n\t" \
"ds_write_b64 v157, v[60:61] offset:57344\n\t" \
"s_waitcnt lgkmcnt(0)\n\t" \
"s_barrier\n\t" \
"v_lshl_add_u32 v5, v2, 2, v3\n\t" \
"v_and_b32 v6, 7, v5\n\t" \
"v_lshlrev_b32 v5, 9, v5\n\t" \
"v_lshl_add_u32 v5, v4, 3, v5\n\t" \
"s_add_u32 s60, s36, 0\n\t" \
"s_and_b32 s60, s60, 3\n\t" \
"s_lshl_b32 s60, s60, 2\n\t" \
"s_add_u32 s60, s60, s32\n\t" \
"v_xor_b32 v7, s60, v6\n\t" \
"v_lshl_add_u32 v208, v7, 5, v5\n\t" \
"v_add_u32 v212, 0x10000, v208\n\t" \
"v_add_u32 v216, 0x20000, v208\n\t" \
"s_add_u32 s60, s36, 1\n\t" \
"s_and_b32 s60, s60, 3\n\t" \
"s_lshl_b32 s60, s60, 2\n\t" \
"s_add_u32 s60, s60, s32\n\t" \
"v_xor_b32 v7, s60, v6\n\t" \
"v_lshl_add_u32 v209, v7, 5, v5\n\t" \
"v_add_u32 v213, 0x10000, v209\n\t" \
"v_add_u32 v217, 0x20000, v209\n\t" \
"s_add_u32 s60, s36, 2\n\t" \
"s_and_b32 s60, s60, 3\n\t" \
"s_lshl_b32 s60, s60, 2\n\t" \
"s_add_u32 s60, s60, s32\n\t" \
"v_xor_b32 v7, s60, v6\n\t" \
"v_lshl_add_u32 v210, v7, 5, v5\n\t" \
"v_add_u32 v214, 0x10000, v210\n\t" \
"v_add_u32 v218, 0x20000, v210\n\t" \
"s_add_u32 s60, s36, 3\n\t" \
"s_and_b32 s60, s60, 3\n\t" \
"s_lshl_b32 s60, s60, 2\n\t" \
"s_add_u32 s60, s60, s32\n\t" \
"v_xor_b32 v7, s60, v6\n\t" \
"v_lshl_add_u32 v211, v7, 5, v5\n\t" \
"v_add_u32 v215, 0x10000, v211\n\t" \
"v_add_u32 v219, 0x20000, v211\n\t" \
"v_lshlrev_b32 v220, 4, v0\n\t" \
"v_add_u32 v221, 0x10000, v220\n\t" \
"v_add_u32 v222, 0x20000, v220\n\t" \
"v_lshrrev_b32 v5, 5, v0\n\t" \
"v_and_b32 v6, 31, v0\n\t" \
"v_lshlrev_b32 v7, 11, v5\n\t" \
"s_lshl_b32 s60, s71, 15\n\t" \
"v_add_u32 v7, s60, v7\n\t" \
"v_lshlrev_b32 v5, 1, v5\n\t" \
"v_xor_b32 v6, v6, v5\n\t" \
"v_xor_b32 v5, 0, v6\n\t" \
"v_lshl_add_u32 v5, v5, 4, v7\n\t" \
"v_add_u32 v225, 0, v5\n\t" \
"v_xor_b32 v5, 4, v6\n\t" \
"v_lshl_add_u32 v5, v5, 4, v7\n\t" \
"v_add_u32 v226, 3072, v5\n\t" \
"v_xor_b32 v5, 8, v6\n\t" \
"v_lshl_add_u32 v5, v5, 4, v7\n\t" \
"v_add_u32 v227, 6144, v5\n\t" \
"v_xor_b32 v5, 12, v6\n\t" \
"v_lshl_add_u32 v5, v5, 4, v7\n\t" \
"v_add_u32 v228, 9216, v5\n\t" \
"s_lshl_b32 s60, s32, 2\n\t" \
"s_add_u32 s60, s60, 0x7c20\n\t" \
"v_mov_b32 v229, 0x7c00\n\t" \
"v_mov_b32 v230, s60\n\t" \
"s_lshl_b32 s60, s71, 2\n\t" \
"s_add_u32 s60, s60, 0x7c30\n\t" \
"v_mov_b32 v231, s60\n\t" \
"s_barrier\n\t" \
"s_cmp_eq_u32 s74, 1\n\t" \
"s_cbranch_scc0 L_r37_%=\n\t" \
"s_add_u32 s64, s70, 2\n\t" \
"s_add_u32 s61, s71, s64\n\t" \
"s_lshr_b32 s63, s61, 4\n\t" \
"s_mul_i32 s63, s63, 0x100000\n\t" \
"s_add_u32 s61, s61, s81\n\t" \
"s_and_b32 s61, s61, 15\n\t" \
"s_lshl_b32 s61, s61, 14\n\t" \
"s_add_u32 s61, s61, s63\n\t" \
"s_add_u32 m0, s78, s79\n\t" \
"s_nop 0\n\t" \
"buffer_load_dwordx4 v232, s[12:15], s61 offen offset:0 nt lds\n\t" \
"buffer_load_dwordx4 v233, s[12:15], s61 offen offset:1024 nt lds\n\t" \
"buffer_load_dwordx4 v234, s[12:15], s61 offen offset:2048 nt lds\n\t" \
"buffer_load_dwordx4 v235, s[12:15], s61 offen offset:3072 nt lds\n\t" \
"s_add_u32 m0, m0, 4096\n\t" \
"s_add_u32 s61, s61, 4096\n\t" \
"buffer_load_dwordx4 v232, s[12:15], s61 offen offset:0 nt lds\n\t" \
"buffer_load_dwordx4 v233, s[12:15], s61 offen offset:1024 nt lds\n\t" \
"buffer_load_dwordx4 v234, s[12:15], s61 offen offset:2048 nt lds\n\t" \
"buffer_load_dwordx4 v235, s[12:15], s61 offen offset:3072 nt lds\n\t" \
"s_mov_b32 s61, s64\n\t" \
"s_lshr_b32 s63, s61, 4\n\t" \
"s_mul_i32 s63, s63, 0x100000\n\t" \
"s_add_u32 s61, s61, s81\n\t" \
"s_and_b32 s61, s61, 15\n\t" \
"s_lshl_b32 s61, s61, 14\n\t" \
"s_add_u32 s61, s61, s63\n\t" \
"s_add_u32 m0, s73, s79\n\t" \
"s_nop 0\n\t" \
"buffer_load_dwordx4 v236, s[16:19], s61 offen offset:0 nt lds\n\t" \
"buffer_load_dwordx4 v237, s[16:19], s61 offen offset:1024 nt lds\n\t" \
"buffer_load_dwordx4 v236, s[16:19], s61 offen offset:2048 nt lds\n\t" \
"buffer_load_dwordx4 v237, s[16:19], s61 offen offset:3072 nt lds\n\t" \
"s_add_u32 m0, m0, 4096\n\t" \
"s_add_u32 s61, s61, 4096\n\t" \
"buffer_load_dwordx4 v236, s[16:19], s61 offen offset:0 nt lds\n\t" \
"buffer_load_dwordx4 v237, s[16:19], s61 offen offset:1024 nt lds\n\t" \
"buffer_load_dwordx4 v236, s[16:19], s61 offen offset:2048 nt lds\n\t" \
"buffer_load_dwordx4 v237, s[16:19], s61 offen offset:3072 nt lds\n\t" \
"s_cmp_eq_u32 s79, 0x10000\n\t" \
"s_cselect_b32 s67, s79, 0xffff8000\n\t" \
"s_add_u32 s79, s79, s67\n\t" \
"s_waitcnt vmcnt(16)\n\t" \
"L_r37_%=:\n\t" \
"s_add_u32 s70, s70, 1\n\t" \
"v_mov_b32 v242, 0\n\t" \
"v_mov_b32 v243, 0\n\t" \
"v_mov_b32 v244, 0\n\t" \
"v_mov_b32 v245, 0\n\t" \
"v_mov_b32 v246, 8\n\t" \
"ds_write_b128 v229, v[242:245] offset:0\n\t" \
"ds_write_b128 v229, v[242:245] offset:16\n\t" \
"ds_write_b128 v229, v[242:245] offset:32\n\t" \
"ds_write_b128 v229, v[242:245] offset:48\n\t" \
"ds_write_b32 v229, v246 offset:12\n\t" \
"ds_write_b32 v229, v246 offset:4\n\t" \
"s_waitcnt lgkmcnt(0)\n\t" \
"L_p2_%=:\n\t" \
"s_barrier\n\t" \
"s_cmp_eq_u32 s74, 1\n\t" \
"s_cbranch_scc0 L_r38_%=\n\t" \
"s_add_u32 s64, s70, 2\n\t" \
"s_add_u32 s61, s71, s64\n\t" \
"s_lshr_b32 s63, s61, 4\n\t" \
"s_mul_i32 s63, s63, 0x100000\n\t" \
"s_add_u32 s61, s61, s81\n\t" \
"s_and_b32 s61, s61, 15\n\t" \
"s_lshl_b32 s61, s61, 14\n\t" \
"s_add_u32 s61, s61, s63\n\t" \
"s_add_u32 m0, s78, s79\n\t" \
"s_nop 0\n\t" \
"buffer_load_dwordx4 v232, s[12:15], s61 offen offset:0 nt lds\n\t" \
"buffer_load_dwordx4 v233, s[12:15], s61 offen offset:1024 nt lds\n\t" \
"buffer_load_dwordx4 v234, s[12:15], s61 offen offset:2048 nt lds\n\t" \
"buffer_load_dwordx4 v235, s[12:15], s61 offen offset:3072 nt lds\n\t" \
"s_add_u32 m0, m0, 4096\n\t" \
"s_add_u32 s61, s61, 4096\n\t" \
"buffer_load_dwordx4 v232, s[12:15], s61 offen offset:0 nt lds\n\t" \
"buffer_load_dwordx4 v233, s[12:15], s61 offen offset:1024 nt lds\n\t" \
"buffer_load_dwordx4 v234, s[12:15], s61 offen offset:2048 nt lds\n\t" \
"buffer_load_dwordx4 v235, s[12:15], s61 offen offset:3072 nt lds\n\t" \
"s_mov_b32 s61, s64\n\t" \
"s_lshr_b32 s63, s61, 4\n\t" \
"s_mul_i32 s63, s63, 0x100000\n\t" \
"s_add_u32 s61, s61, s81\n\t" \
"s_and_b32 s61, s61, 15\n\t" \
"s_lshl_b32 s61, s61, 14\n\t" \
"s_add_u32 s61, s61, s63\n\t" \
"s_add_u32 m0, s73, s79\n\t" \
"s_nop 0\n\t" \
"buffer_load_dwordx4 v236, s[16:19], s61 offen offset:0 nt lds\n\t" \
"buffer_load_dwordx4 v237, s[16:19], s61 offen offset:1024 nt lds\n\t" \
"buffer_load_dwordx4 v236, s[16:19], s61 offen offset:2048 nt lds\n\t" \
"buffer_load_dwordx4 v237, s[16:19], s61 offen offset:3072 nt lds\n\t" \
"s_add_u32 m0, m0, 4096\n\t" \
"s_add_u32 s61, s61, 4096\n\t" \
"buffer_load_dwordx4 v236, s[16:19], s61 offen offset:0 nt lds\n\t" \
"buffer_load_dwordx4 v237, s[16:19], s61 offen offset:1024 nt lds\n\t" \
"buffer_load_dwordx4 v236, s[16:19], s61 offen offset:2048 nt lds\n\t" \
"buffer_load_dwordx4 v237, s[16:19], s61 offen offset:3072 nt lds\n\t" \
"s_cmp_eq_u32 s79, 0x10000\n\t" \
"s_cselect_b32 s67, s79, 0xffff8000\n\t" \
"s_add_u32 s79, s79, s67\n\t" \
"s_waitcnt vmcnt(16)\n\t" \
"L_r38_%=:\n\t" \
"s_add_u32 s70, s70, 1\n\t" \
"s_cmp_lt_u32 s70, 27\n\t" \
"s_cbranch_scc1 L_p2_%=\n\t" \
"s_barrier\n\t" \
"s_cmp_eq_u32 s74, 0\n\t" \
"s_cbranch_scc0 L_r39_%=\n\t" \
"s_mov_b32 s60, s39\n\t" \
"s_mul_i32 s61, s60, 28672\n\t" \
"s_add_u32 s61, s61, s82\n\t" \
"s_lshl_b32 s62, s60, 18\n\t" \
"s_add_u32 m0, s78, 0x8000\n\t" \
"s_nop 0\n\t" \
"buffer_load_dwordx4 v225, s[20:23], s62 offen offset:0 lds\n\t" \
"buffer_load_dwordx4 v226, s[20:23], s62 offen offset:1024 lds\n\t" \
"buffer_load_dwordx4 v227, s[20:23], s62 offen offset:2048 lds\n\t" \
"buffer_load_dwordx4 v228, s[20:23], s62 offen offset:3072 lds\n\t" \
"s_add_u32 m0, m0, 4096\n\t" \
"s_add_u32 s62, s62, 0x4000\n\t" \
"buffer_load_dwordx4 v225, s[20:23], s62 offen offset:0 lds\n\t" \
"buffer_load_dwordx4 v226, s[20:23], s62 offen offset:1024 lds\n\t" \
"buffer_load_dwordx4 v227, s[20:23], s62 offen offset:2048 lds\n\t" \
"buffer_load_dwordx4 v228, s[20:23], s62 offen offset:3072 lds\n\t" \
"s_mov_b32 s60, s39\n\t" \
"s_mul_i32 s61, s60, 28672\n\t" \
"s_add_u32 s61, s61, s82\n\t" \
"s_lshl_b32 s62, s60, 18\n\t" \
"s_add_u32 s61, s61, 7168\n\t" \
"s_add_u32 s62, s62, 0x10000\n\t" \
"s_add_u32 m0, s78, 0xc000\n\t" \
"s_nop 0\n\t" \
"buffer_load_dwordx4 v225, s[20:23], s62 offen offset:0 lds\n\t" \
"buffer_load_dwordx4 v226, s[20:23], s62 offen offset:1024 lds\n\t" \
"buffer_load_dwordx4 v227, s[20:23], s62 offen offset:2048 lds\n\t" \
"buffer_load_dwordx4 v228, s[20:23], s62 offen offset:3072 lds\n\t" \
"s_add_u32 m0, m0, 4096\n\t" \
"s_add_u32 s62, s62, 0x4000\n\t" \
"buffer_load_dwordx4 v225, s[20:23], s62 offen offset:0 lds\n\t" \
"buffer_load_dwordx4 v226, s[20:23], s62 offen offset:1024 lds\n\t" \
"buffer_load_dwordx4 v227, s[20:23], s62 offen offset:2048 lds\n\t" \
"buffer_load_dwordx4 v228, s[20:23], s62 offen offset:3072 lds\n\t" \
"s_waitcnt vmcnt(8)\n\t" \
"L_r39_%=:\n\t" \
"s_cmp_eq_u32 s74, 1\n\t" \
"s_cbranch_scc0 L_r40_%=\n\t" \
"s_add_u32 s64, s70, 2\n\t" \
"s_add_u32 s61, s71, s64\n\t" \
"s_lshr_b32 s63, s61, 4\n\t" \
"s_mul_i32 s63, s63, 0x100000\n\t" \
"s_add_u32 s61, s61, s81\n\t" \
"s_and_b32 s61, s61, 15\n\t" \
"s_lshl_b32 s61, s61, 14\n\t" \
"s_add_u32 s61, s61, s63\n\t" \
"s_add_u32 m0, s78, s79\n\t" \
"s_nop 0\n\t" \
"buffer_load_dwordx4 v232, s[12:15], s61 offen offset:0 nt lds\n\t" \
"buffer_load_dwordx4 v233, s[12:15], s61 offen offset:1024 nt lds\n\t" \
"buffer_load_dwordx4 v234, s[12:15], s61 offen offset:2048 nt lds\n\t" \
"buffer_load_dwordx4 v235, s[12:15], s61 offen offset:3072 nt lds\n\t" \
"s_add_u32 m0, m0, 4096\n\t" \
"s_add_u32 s61, s61, 4096\n\t" \
"buffer_load_dwordx4 v232, s[12:15], s61 offen offset:0 nt lds\n\t" \
"buffer_load_dwordx4 v233, s[12:15], s61 offen offset:1024 nt lds\n\t" \
"buffer_load_dwordx4 v234, s[12:15], s61 offen offset:2048 nt lds\n\t" \
"buffer_load_dwordx4 v235, s[12:15], s61 offen offset:3072 nt lds\n\t" \
"s_mov_b32 s61, s64\n\t" \
"s_lshr_b32 s63, s61, 4\n\t" \
"s_mul_i32 s63, s63, 0x100000\n\t" \
"s_add_u32 s61, s61, s81\n\t" \
"s_and_b32 s61, s61, 15\n\t" \
"s_lshl_b32 s61, s61, 14\n\t" \
"s_add_u32 s61, s61, s63\n\t" \
"s_add_u32 m0, s73, s79\n\t" \
"s_nop 0\n\t" \
"buffer_load_dwordx4 v236, s[16:19], s61 offen offset:0 nt lds\n\t" \
"buffer_load_dwordx4 v237, s[16:19], s61 offen offset:1024 nt lds\n\t" \
"buffer_load_dwordx4 v236, s[16:19], s61 offen offset:2048 nt lds\n\t" \
"buffer_load_dwordx4 v237, s[16:19], s61 offen offset:3072 nt lds\n\t" \
"s_add_u32 m0, m0, 4096\n\t" \
"s_add_u32 s61, s61, 4096\n\t" \
"buffer_load_dwordx4 v236, s[16:19], s61 offen offset:0 nt lds\n\t" \
"buffer_load_dwordx4 v237, s[16:19], s61 offen offset:1024 nt lds\n\t" \
"buffer_load_dwordx4 v236, s[16:19], s61 offen offset:2048 nt lds\n\t" \
"buffer_load_dwordx4 v237, s[16:19], s61 offen offset:3072 nt lds\n\t" \
"s_cmp_eq_u32 s79, 0x10000\n\t" \
"s_cselect_b32 s67, s79, 0xffff8000\n\t" \
"s_add_u32 s79, s79, s67\n\t" \
"s_waitcnt vmcnt(16)\n\t" \
"L_r40_%=:\n\t" \
"s_add_u32 s70, s70, 1\n\t" \
"s_barrier\n\t" \
"ds_read_b64_tr_b16 v[120:121], v208 offset:32768\n\t" \
"ds_read_b64_tr_b16 v[122:123], v208 offset:40960\n\t" \
"ds_read_b64_tr_b16 v[124:125], v209 offset:32768\n\t" \
"ds_read_b64_tr_b16 v[126:127], v209 offset:40960\n\t" \
"ds_read_b64_tr_b16 v[128:129], v210 offset:32768\n\t" \
"ds_read_b64_tr_b16 v[130:131], v210 offset:40960\n\t" \
"ds_read_b64_tr_b16 v[132:133], v211 offset:32768\n\t" \
"ds_read_b64_tr_b16 v[134:135], v211 offset:40960\n\t" \
"ds_read_b128 v[152:155], v220 offset:0\n\t" \
"ds_read_b128 v[156:159], v220 offset:1024\n\t" \
"ds_read_b128 v[160:163], v220 offset:2048\n\t" \
"ds_read_b128 v[164:167], v220 offset:3072\n\t" \
"ds_read_b128 v[168:171], v220 offset:4096\n\t" \
"ds_read_b128 v[172:175], v220 offset:5120\n\t" \
"ds_read_b128 v[176:179], v220 offset:6144\n\t" \
"s_cmp_eq_u32 s74, 0\n\t" \
"s_cbranch_scc0 L_r41_%=\n\t" \
"s_waitcnt vmcnt(0)\n\t" \
"L_r41_%=:\n\t" \
"s_cmp_eq_u32 s74, 1\n\t" \
"s_cbranch_scc0 L_r42_%=\n\t" \
"s_add_u32 s64, s70, 2\n\t" \
"s_add_u32 s61, s71, s64\n\t" \
"s_lshr_b32 s63, s61, 4\n\t" \
"s_mul_i32 s63, s63, 0x100000\n\t" \
"s_add_u32 s61, s61, s81\n\t" \
"s_and_b32 s61, s61, 15\n\t" \
"s_lshl_b32 s61, s61, 14\n\t" \
"s_add_u32 s61, s61, s63\n\t" \
"s_add_u32 m0, s78, s79\n\t" \
"s_nop 0\n\t" \
"buffer_load_dwordx4 v232, s[12:15], s61 offen offset:0 nt lds\n\t" \
"buffer_load_dwordx4 v233, s[12:15], s61 offen offset:1024 nt lds\n\t" \
"buffer_load_dwordx4 v234, s[12:15], s61 offen offset:2048 nt lds\n\t" \
"buffer_load_dwordx4 v235, s[12:15], s61 offen offset:3072 nt lds\n\t" \
"s_add_u32 m0, m0, 4096\n\t" \
"s_add_u32 s61, s61, 4096\n\t" \
"buffer_load_dwordx4 v232, s[12:15], s61 offen offset:0 nt lds\n\t" \
"buffer_load_dwordx4 v233, s[12:15], s61 offen offset:1024 nt lds\n\t" \
"buffer_load_dwordx4 v234, s[12:15], s61 offen offset:2048 nt lds\n\t" \
"buffer_load_dwordx4 v235, s[12:15], s61 offen offset:3072 nt lds\n\t" \
"s_mov_b32 s61, s64\n\t" \
"s_lshr_b32 s63, s61, 4\n\t" \
"s_mul_i32 s63, s63, 0x100000\n\t" \
"s_add_u32 s61, s61, s81\n\t" \
"s_and_b32 s61, s61, 15\n\t" \
"s_lshl_b32 s61, s61, 14\n\t" \
"s_add_u32 s61, s61, s63\n\t" \
"s_add_u32 m0, s73, s79\n\t" \
"s_nop 0\n\t" \
"buffer_load_dwordx4 v236, s[16:19], s61 offen offset:0 nt lds\n\t" \
"buffer_load_dwordx4 v237, s[16:19], s61 offen offset:1024 nt lds\n\t" \
"buffer_load_dwordx4 v236, s[16:19], s61 offen offset:2048 nt lds\n\t" \
"buffer_load_dwordx4 v237, s[16:19], s61 offen offset:3072 nt lds\n\t" \
"s_add_u32 m0, m0, 4096\n\t" \
"s_add_u32 s61, s61, 4096\n\t" \
"buffer_load_dwordx4 v236, s[16:19], s61 offen offset:0 nt lds\n\t" \
"buffer_load_dwordx4 v237, s[16:19], s61 offen offset:1024 nt lds\n\t" \
"buffer_load_dwordx4 v236, s[16:19], s61 offen offset:2048 nt lds\n\t" \
"buffer_load_dwordx4 v237, s[16:19], s61 offen offset:3072 nt lds\n\t" \
"s_cmp_eq_u32 s79, 0x10000\n\t" \
"s_cselect_b32 s67, s79, 0xffff8000\n\t" \
"s_add_u32 s79, s79, s67\n\t" \
"s_waitcnt vmcnt(16)\n\t" \
"L_r42_%=:\n\t" \
"s_add_u32 s70, s70, 1\n\t" \
"s_waitcnt lgkmcnt(0)\n\t" \
"s_barrier\n\t" \
"s_cmp_eq_u32 s74, 0\n\t" \
"s_cbranch_scc0 L_r43_%=\n\t" \
"s_mov_b32 s60, s39\n\t" \
"s_mul_i32 s61, s60, 28672\n\t" \
"s_add_u32 s61, s61, s82\n\t" \
"s_lshl_b32 s62, s60, 18\n\t" \
"s_add_u32 s61, s61, 14336\n\t" \
"s_add_u32 s62, s62, 0x20000\n\t" \
"s_add_u32 m0, s78, 0x8000\n\t" \
"s_nop 0\n\t" \
"buffer_load_dwordx4 v225, s[20:23], s62 offen offset:0 lds\n\t" \
"buffer_load_dwordx4 v226, s[20:23], s62 offen offset:1024 lds\n\t" \
"buffer_load_dwordx4 v227, s[20:23], s62 offen offset:2048 lds\n\t" \
"buffer_load_dwordx4 v228, s[20:23], s62 offen offset:3072 lds\n\t" \
"s_add_u32 m0, m0, 4096\n\t" \
"s_add_u32 s62, s62, 0x4000\n\t" \
"buffer_load_dwordx4 v225, s[20:23], s62 offen offset:0 lds\n\t" \
"buffer_load_dwordx4 v226, s[20:23], s62 offen offset:1024 lds\n\t" \
"buffer_load_dwordx4 v227, s[20:23], s62 offen offset:2048 lds\n\t" \
"buffer_load_dwordx4 v228, s[20:23], s62 offen offset:3072 lds\n\t" \
"L_r43_%=:\n\t" \
"s_cmp_eq_u32 s74, 1\n\t" \
"s_cbranch_scc0 L_r44_%=\n\t" \
"s_cmp_eq_u32 s71, 1\n\t" \
"s_cselect_b32 s60, 0x40000000, 0\n\t" \
"v_or_b32 v240, s60, v232\n\t" \
"v_or_b32 v241, s60, v233\n\t" \
"v_or_b32 v242, s60, v234\n\t" \
"v_or_b32 v243, s60, v235\n\t" \
"s_mov_b32 s61, 31\n\t" \
"s_lshr_b32 s63, s61, 4\n\t" \
"s_mul_i32 s63, s63, 0x100000\n\t" \
"s_add_u32 s61, s61, s81\n\t" \
"s_and_b32 s61, s61, 15\n\t" \
"s_lshl_b32 s61, s61, 14\n\t" \
"s_add_u32 s61, s61, s63\n\t" \
"s_add_u32 m0, s78, s79\n\t" \
"s_nop 0\n\t" \
"buffer_load_dwordx4 v240, s[12:15], s61 offen offset:0 nt lds\n\t" \
"buffer_load_dwordx4 v241, s[12:15], s61 offen offset:1024 nt lds\n\t" \
"buffer_load_dwordx4 v242, s[12:15], s61 offen offset:2048 nt lds\n\t" \
"buffer_load_dwordx4 v243, s[12:15], s61 offen offset:3072 nt lds\n\t" \
"s_add_u32 m0, m0, 4096\n\t" \
"s_add_u32 s61, s61, 4096\n\t" \
"buffer_load_dwordx4 v240, s[12:15], s61 offen offset:0 nt lds\n\t" \
"buffer_load_dwordx4 v241, s[12:15], s61 offen offset:1024 nt lds\n\t" \
"buffer_load_dwordx4 v242, s[12:15], s61 offen offset:2048 nt lds\n\t" \
"buffer_load_dwordx4 v243, s[12:15], s61 offen offset:3072 nt lds\n\t" \
"s_mov_b32 s61, 31\n\t" \
"s_lshr_b32 s63, s61, 4\n\t" \
"s_mul_i32 s63, s63, 0x100000\n\t" \
"s_add_u32 s61, s61, s81\n\t" \
"s_and_b32 s61, s61, 15\n\t" \
"s_lshl_b32 s61, s61, 14\n\t" \
"s_add_u32 s61, s61, s63\n\t" \
"s_add_u32 m0, s73, s79\n\t" \
"s_nop 0\n\t" \
"buffer_load_dwordx4 v236, s[16:19], s61 offen offset:0 nt lds\n\t" \
"buffer_load_dwordx4 v237, s[16:19], s61 offen offset:1024 nt lds\n\t" \
"buffer_load_dwordx4 v236, s[16:19], s61 offen offset:2048 nt lds\n\t" \
"buffer_load_dwordx4 v237, s[16:19], s61 offen offset:3072 nt lds\n\t" \
"s_add_u32 m0, m0, 4096\n\t" \
"s_add_u32 s61, s61, 4096\n\t" \
"buffer_load_dwordx4 v236, s[16:19], s61 offen offset:0 nt lds\n\t" \
"buffer_load_dwordx4 v237, s[16:19], s61 offen offset:1024 nt lds\n\t" \
"buffer_load_dwordx4 v236, s[16:19], s61 offen offset:2048 nt lds\n\t" \
"buffer_load_dwordx4 v237, s[16:19], s61 offen offset:3072 nt lds\n\t" \
"L_r44_%=:\n\t" \
"v_mfma_f32_16x16x32_bf16 v[8:11], v[120:123], v[152:155], 0\n\t" \
"ds_read_b64_tr_b16 v[136:137], v208 offset:49152\n\t" \
"v_mfma_f32_16x16x32_bf16 v[12:15], v[120:123], v[156:159], 0\n\t" \
"ds_read_b64_tr_b16 v[138:139], v208 offset:57344\n\t" \
"v_mfma_f32_16x16x32_bf16 v[16:19], v[120:123], v[160:163], 0\n\t" \
"ds_read_b64_tr_b16 v[140:141], v209 offset:49152\n\t" \
"v_mfma_f32_16x16x32_bf16 v[20:23], v[120:123], v[164:167], 0\n\t" \
"ds_read_b64_tr_b16 v[142:143], v209 offset:57344\n\t" \
"v_mfma_f32_16x16x32_bf16 v[24:27], v[120:123], v[168:171], 0\n\t" \
"ds_read_b64_tr_b16 v[144:145], v210 offset:49152\n\t" \
"v_mfma_f32_16x16x32_bf16 v[28:31], v[120:123], v[172:175], 0\n\t" \
"ds_read_b64_tr_b16 v[146:147], v210 offset:57344\n\t" \
"v_mfma_f32_16x16x32_bf16 v[32:35], v[120:123], v[176:179], 0\n\t" \
"ds_read_b64_tr_b16 v[148:149], v211 offset:49152\n\t" \
"v_mfma_f32_16x16x32_bf16 v[36:39], v[124:127], v[152:155], 0\n\t" \
"ds_read_b64_tr_b16 v[150:151], v211 offset:57344\n\t" \
"v_mfma_f32_16x16x32_bf16 v[40:43], v[124:127], v[156:159], 0\n\t" \
"ds_read_b128 v[180:183], v220 offset:8192\n\t" \
"v_mfma_f32_16x16x32_bf16 v[44:47], v[124:127], v[160:163], 0\n\t" \
"ds_read_b128 v[184:187], v220 offset:9216\n\t" \
"v_mfma_f32_16x16x32_bf16 v[48:51], v[124:127], v[164:167], 0\n\t" \
"ds_read_b128 v[188:191], v220 offset:10240\n\t" \
"v_mfma_f32_16x16x32_bf16 v[52:55], v[124:127], v[168:171], 0\n\t" \
"ds_read_b128 v[192:195], v220 offset:11264\n\t" \
"v_mfma_f32_16x16x32_bf16 v[56:59], v[124:127], v[172:175], 0\n\t" \
"ds_read_b128 v[196:199], v220 offset:12288\n\t" \
"v_mfma_f32_16x16x32_bf16 v[60:63], v[124:127], v[176:179], 0\n\t" \
"ds_read_b128 v[200:203], v220 offset:13312\n\t" \
"v_mfma_f32_16x16x32_bf16 v[64:67], v[128:131], v[152:155], 0\n\t" \
"ds_read_b128 v[204:207], v220 offset:14336\n\t" \
"v_mfma_f32_16x16x32_bf16 v[68:71], v[128:131], v[156:159], 0\n\t" \
"v_mfma_f32_16x16x32_bf16 v[72:75], v[128:131], v[160:163], 0\n\t" \
"v_mfma_f32_16x16x32_bf16 v[76:79], v[128:131], v[164:167], 0\n\t" \
"v_mfma_f32_16x16x32_bf16 v[80:83], v[128:131], v[168:171], 0\n\t" \
"v_mfma_f32_16x16x32_bf16 v[84:87], v[128:131], v[172:175], 0\n\t" \
"v_mfma_f32_16x16x32_bf16 v[88:91], v[128:131], v[176:179], 0\n\t" \
"v_mfma_f32_16x16x32_bf16 v[92:95], v[132:135], v[152:155], 0\n\t" \
"v_mfma_f32_16x16x32_bf16 v[96:99], v[132:135], v[156:159], 0\n\t" \
"v_mfma_f32_16x16x32_bf16 v[100:103], v[132:135], v[160:163], 0\n\t" \
"v_mfma_f32_16x16x32_bf16 v[104:107], v[132:135], v[164:167], 0\n\t" \
"v_mfma_f32_16x16x32_bf16 v[108:111], v[132:135], v[168:171], 0\n\t" \
"v_mfma_f32_16x16x32_bf16 v[112:115], v[132:135], v[172:175], 0\n\t" \
"v_mfma_f32_16x16x32_bf16 v[116:119], v[132:135], v[176:179], 0\n\t" \
"s_cmp_eq_u32 s74, 1\n\t" \
"s_cbranch_scc0 L_r45_%=\n\t" \
"s_waitcnt vmcnt(16)\n\t" \
"L_r45_%=:\n\t" \
"s_waitcnt lgkmcnt(0)\n\t" \
"s_barrier\n\t" \
"s_cmp_eq_u32 s74, 1\n\t" \
"s_cbranch_scc0 L_r46_%=\n\t" \
"s_mov_b32 s60, s39\n\t" \
"s_mul_i32 s61, s60, 28672\n\t" \
"s_add_u32 s61, s61, s82\n\t" \
"s_lshl_b32 s62, s60, 18\n\t" \
"s_add_u32 s61, s61, 21504\n\t" \
"s_add_u32 s62, s62, 0x30000\n\t" \
"s_add_u32 m0, s78, 0xc000\n\t" \
"s_nop 0\n\t" \
"buffer_load_dwordx4 v225, s[20:23], s62 offen offset:0 lds\n\t" \
"buffer_load_dwordx4 v226, s[20:23], s62 offen offset:1024 lds\n\t" \
"buffer_load_dwordx4 v227, s[20:23], s62 offen offset:2048 lds\n\t" \
"buffer_load_dwordx4 v228, s[20:23], s62 offen offset:3072 lds\n\t" \
"s_add_u32 m0, m0, 4096\n\t" \
"s_add_u32 s62, s62, 0x4000\n\t" \
"buffer_load_dwordx4 v225, s[20:23], s62 offen offset:0 lds\n\t" \
"buffer_load_dwordx4 v226, s[20:23], s62 offen offset:1024 lds\n\t" \
"buffer_load_dwordx4 v227, s[20:23], s62 offen offset:2048 lds\n\t" \
"buffer_load_dwordx4 v228, s[20:23], s62 offen offset:3072 lds\n\t" \
"s_mov_b32 s60, s39\n\t" \
"s_add_u32 s60, s60, 4\n\t" \
"s_mul_i32 s61, s60, 28672\n\t" \
"s_add_u32 s61, s61, s82\n\t" \
"s_lshl_b32 s62, s60, 18\n\t" \
"s_add_u32 m0, s78, 0x24000\n\t" \
"s_nop 0\n\t" \
"buffer_load_dwordx4 v225, s[20:23], s62 offen offset:0 lds\n\t" \
"buffer_load_dwordx4 v226, s[20:23], s62 offen offset:1024 lds\n\t" \
"buffer_load_dwordx4 v227, s[20:23], s62 offen offset:2048 lds\n\t" \
"buffer_load_dwordx4 v228, s[20:23], s62 offen offset:3072 lds\n\t" \
"s_add_u32 m0, m0, 4096\n\t" \
"s_add_u32 s62, s62, 0x4000\n\t" \
"buffer_load_dwordx4 v225, s[20:23], s62 offen offset:0 lds\n\t" \
"buffer_load_dwordx4 v226, s[20:23], s62 offen offset:1024 lds\n\t" \
"buffer_load_dwordx4 v227, s[20:23], s62 offen offset:2048 lds\n\t" \
"buffer_load_dwordx4 v228, s[20:23], s62 offen offset:3072 lds\n\t" \
"s_waitcnt vmcnt(16)\n\t" \
"L_r46_%=:\n\t" \
"s_cmp_eq_u32 s74, 0\n\t" \
"s_cbranch_scc0 L_r47_%=\n\t" \
"s_lshl_b32 s60, s71, 14\n\t" \
"v_add_u32 v5, s60, v220\n\t" \
"s_mul_i32 s60, s71, 14336\n\t" \
"s_add_u32 s60, s60, s56\n\t" \
"ds_read_b128 v[120:123], v5 offset:0\n\t" \
"ds_read_b128 v[124:127], v5 offset:1024\n\t" \
"ds_read_b128 v[128:131], v5 offset:2048\n\t" \
"ds_read_b128 v[132:135], v5 offset:3072\n\t" \
"ds_read_b128 v[152:155], v5 offset:4096\n\t" \
"ds_read_b128 v[156:159], v5 offset:5120\n\t" \
"ds_read_b128 v[160:163], v5 offset:6144\n\t" \
"s_waitcnt lgkmcnt(6)\n\t" \
"buffer_store_dwordx4 v[120:123], v220, s[24:27], s60 offen offset:0 sc1\n\t" \
"s_waitcnt lgkmcnt(5)\n\t" \
"buffer_store_dwordx4 v[124:127], v220, s[24:27], s60 offen offset:1024 sc1\n\t" \
"s_waitcnt lgkmcnt(4)\n\t" \
"buffer_store_dwordx4 v[128:131], v220, s[24:27], s60 offen offset:2048 sc1\n\t" \
"s_waitcnt lgkmcnt(3)\n\t" \
"buffer_store_dwordx4 v[132:135], v220, s[24:27], s60 offen offset:3072 sc1\n\t" \
"s_add_u32 s60, s60, 4096\n\t" \
"s_waitcnt lgkmcnt(2)\n\t" \
"buffer_store_dwordx4 v[152:155], v220, s[24:27], s60 offen offset:0 sc1\n\t" \
"s_waitcnt lgkmcnt(1)\n\t" \
"buffer_store_dwordx4 v[156:159], v220, s[24:27], s60 offen offset:1024 sc1\n\t" \
"s_waitcnt lgkmcnt(0)\n\t" \
"buffer_store_dwordx4 v[160:163], v220, s[24:27], s60 offen offset:2048 sc1\n\t" \
"s_add_u32 s60, s60, 3072\n\t" \
"ds_read_b128 v[152:155], v5 offset:8192\n\t" \
"ds_read_b128 v[156:159], v5 offset:9216\n\t" \
"ds_read_b128 v[160:163], v5 offset:10240\n\t" \
"ds_read_b128 v[164:167], v5 offset:11264\n\t" \
"ds_read_b128 v[168:171], v5 offset:12288\n\t" \
"ds_read_b128 v[172:175], v5 offset:13312\n\t" \
"ds_read_b128 v[176:179], v5 offset:14336\n\t" \
"s_waitcnt lgkmcnt(6)\n\t" \
"buffer_store_dwordx4 v[152:155], v220, s[24:27], s60 offen offset:0 sc1\n\t" \
"s_waitcnt lgkmcnt(5)\n\t" \
"buffer_store_dwordx4 v[156:159], v220, s[24:27], s60 offen offset:1024 sc1\n\t" \
"s_waitcnt lgkmcnt(4)\n\t" \
"buffer_store_dwordx4 v[160:163], v220, s[24:27], s60 offen offset:2048 sc1\n\t" \
"s_waitcnt lgkmcnt(3)\n\t" \
"buffer_store_dwordx4 v[164:167], v220, s[24:27], s60 offen offset:3072 sc1\n\t" \
"s_add_u32 s60, s60, 4096\n\t" \
"s_waitcnt lgkmcnt(2)\n\t" \
"buffer_store_dwordx4 v[168:171], v220, s[24:27], s60 offen offset:0 sc1\n\t" \
"s_waitcnt lgkmcnt(1)\n\t" \
"buffer_store_dwordx4 v[172:175], v220, s[24:27], s60 offen offset:1024 sc1\n\t" \
"s_waitcnt lgkmcnt(0)\n\t" \
"buffer_store_dwordx4 v[176:179], v220, s[24:27], s60 offen offset:2048 sc1\n\t" \
"s_add_u32 s60, s60, 3072\n\t" \
"L_r47_%=:\n\t" \
"s_waitcnt lgkmcnt(0)\n\t" \
"s_barrier\n\t" \
"s_cmp_eq_u32 s74, 1\n\t" \
"s_cbranch_scc0 L_r48_%=\n\t" \
"s_mov_b32 s60, s39\n\t" \
"s_add_u32 s60, s60, 4\n\t" \
"s_mul_i32 s61, s60, 28672\n\t" \
"s_add_u32 s61, s61, s82\n\t" \
"s_lshl_b32 s62, s60, 18\n\t" \
"s_add_u32 s61, s61, 7168\n\t" \
"s_add_u32 s62, s62, 0x10000\n\t" \
"s_add_u32 m0, s78, 0x1c000\n\t" \
"s_nop 0\n\t" \
"buffer_load_dwordx4 v225, s[20:23], s62 offen offset:0 lds\n\t" \
"buffer_load_dwordx4 v226, s[20:23], s62 offen offset:1024 lds\n\t" \
"buffer_load_dwordx4 v227, s[20:23], s62 offen offset:2048 lds\n\t" \
"buffer_load_dwordx4 v228, s[20:23], s62 offen offset:3072 lds\n\t" \
"s_add_u32 m0, m0, 4096\n\t" \
"s_add_u32 s62, s62, 0x4000\n\t" \
"buffer_load_dwordx4 v225, s[20:23], s62 offen offset:0 lds\n\t" \
"buffer_load_dwordx4 v226, s[20:23], s62 offen offset:1024 lds\n\t" \
"buffer_load_dwordx4 v227, s[20:23], s62 offen offset:2048 lds\n\t" \
"buffer_load_dwordx4 v228, s[20:23], s62 offen offset:3072 lds\n\t" \
"v_lshrrev_b32 v5, 4, v0\n\t" \
"v_bfe_u32 v6, v0, 2, 2\n\t" \
"v_lshl_add_u32 v5, v5, 5, v6\n\t" \
"v_lshlrev_b32 v5, 11, v5\n\t" \
"v_and_b32 v6, 3, v0\n\t" \
"v_lshl_add_u32 v5, v6, 7, v5\n\t" \
"s_lshl_b32 s60, s35, 13\n\t" \
"v_add_u32 v242, s60, v5\n\t" \
"s_cmp_eq_u32 s71, 0\n\t" \
"s_cbranch_scc0 L_t49_%=\n\t" \
"s_mov_b32 s60, s39\n\t" \
"s_add_u32 s60, s60, 4\n\t" \
"s_mul_i32 s61, s60, 28672\n\t" \
"s_add_u32 s61, s61, s82\n\t" \
"s_lshl_b32 s62, s60, 18\n\t" \
"buffer_load_dword v251, v242, s[20:23], s62 offen\n\t" \
"s_add_u32 s60, s37, 2\n\t" \
"s_and_b32 s60, s60, 3\n\t" \
"s_sub_u32 s60, s60, s37\n\t" \
"s_add_u32 s60, s60, s39\n\t" \
"s_mul_i32 s61, s60, 28672\n\t" \
"s_add_u32 s61, s61, s82\n\t" \
"s_lshl_b32 s62, s60, 18\n\t" \
"buffer_load_dword v251, v242, s[20:23], s62 offen\n\t" \
"s_branch L_u49_%=\n\t" \
"L_t49_%=:\n\t" \
"s_add_u32 s60, s37, 1\n\t" \
"s_and_b32 s60, s60, 3\n\t" \
"s_sub_u32 s60, s60, s37\n\t" \
"s_add_u32 s60, s60, s39\n\t" \
"s_mul_i32 s61, s60, 28672\n\t" \
"s_add_u32 s61, s61, s82\n\t" \
"s_lshl_b32 s62, s60, 18\n\t" \
"buffer_load_dword v251, v242, s[20:23], s62 offen\n\t" \
"s_add_u32 s60, s37, 3\n\t" \
"s_and_b32 s60, s60, 3\n\t" \
"s_sub_u32 s60, s60, s37\n\t" \
"s_add_u32 s60, s60, s39\n\t" \
"s_mul_i32 s61, s60, 28672\n\t" \
"s_add_u32 s61, s61, s82\n\t" \
"s_lshl_b32 s62, s60, 18\n\t" \
"buffer_load_dword v251, v242, s[20:23], s62 offen\n\t" \
"L_u49_%=:\n\t" \
"s_waitcnt vmcnt(18)\n\t" \
"L_r48_%=:\n\t" \
"s_cmp_eq_u32 s74, 0\n\t" \
"s_cbranch_scc0 L_r50_%=\n\t" \
"v_lshlrev_b32 v5, 7, v0\n\t" \
"v_add_u32 v6, 0x2000, v5\n\t" \
"v_mov_b32 v7, 0x40000000\n\t" \
"v_cmp_gt_u32 vcc, 48, v0\n\t" \
"s_nop 1\n\t" \
"v_cndmask_b32 v6, v7, v6, vcc\n\t" \
"s_mul_i32 s61, s71, 14336\n\t" \
"s_add_u32 s61, s61, s56\n\t" \
"s_lshl_b32 s60, s71, 3\n\t" \
"s_add_u32 s60, s60, s83\n\t" \
"s_waitcnt vmcnt(0)\n\t" \
"v_mov_b32 v244, s50\n\t" \
"v_mov_b32 v245, s51\n\t" \
"s_mov_b64 exec, 1\n\t" \
"buffer_store_dwordx2 v[244:245], off, s[24:27], s60 offset:64 sc1\n\t" \
"s_mov_b64 exec, -1\n\t" \
"L_r50_%=:\n\t" \
"s_barrier\n\t" \
"s_cmp_eq_u32 s74, 1\n\t" \
"s_cbranch_scc0 L_r51_%=\n\t" \
"s_mov_b32 s60, s39\n\t" \
"s_add_u32 s60, s60, 4\n\t" \
"s_mul_i32 s61, s60, 28672\n\t" \
"s_add_u32 s61, s61, s82\n\t" \
"s_lshl_b32 s62, s60, 18\n\t" \
"s_add_u32 s61, s61, 14336\n\t" \
"s_add_u32 s62, s62, 0x20000\n\t" \
"s_add_u32 m0, s78, 0x14000\n\t" \
"s_nop 0\n\t" \
"buffer_load_dwordx4 v225, s[20:23], s62 offen offset:0 lds\n\t" \
"buffer_load_dwordx4 v226, s[20:23], s62 offen offset:1024 lds\n\t" \
"buffer_load_dwordx4 v227, s[20:23], s62 offen offset:2048 lds\n\t" \
"buffer_load_dwordx4 v228, s[20:23], s62 offen offset:3072 lds\n\t" \
"s_add_u32 m0, m0, 4096\n\t" \
"s_add_u32 s62, s62, 0x4000\n\t" \
"buffer_load_dwordx4 v225, s[20:23], s62 offen offset:0 lds\n\t" \
"buffer_load_dwordx4 v226, s[20:23], s62 offen offset:1024 lds\n\t" \
"buffer_load_dwordx4 v227, s[20:23], s62 offen offset:2048 lds\n\t" \
"buffer_load_dwordx4 v228, s[20:23], s62 offen offset:3072 lds\n\t" \
"L_r51_%=:\n\t" \
"v_mfma_f32_16x16x32_bf16 v[8:11], v[136:139], v[180:183], v[8:11]\n\t" \
"ds_read_b64_tr_b16 v[120:121], v208 offset:32768\n\t" \
"v_mfma_f32_16x16x32_bf16 v[12:15], v[136:139], v[184:187], v[12:15]\n\t" \
"ds_read_b64_tr_b16 v[122:123], v208 offset:40960\n\t" \
"v_mfma_f32_16x16x32_bf16 v[16:19], v[136:139], v[188:191], v[16:19]\n\t" \
"ds_read_b64_tr_b16 v[124:125], v209 offset:32768\n\t" \
"v_mfma_f32_16x16x32_bf16 v[20:23], v[136:139], v[192:195], v[20:23]\n\t" \
"ds_read_b64_tr_b16 v[126:127], v209 offset:40960\n\t" \
"v_mfma_f32_16x16x32_bf16 v[24:27], v[136:139], v[196:199], v[24:27]\n\t" \
"ds_read_b64_tr_b16 v[128:129], v210 offset:32768\n\t" \
"v_mfma_f32_16x16x32_bf16 v[28:31], v[136:139], v[200:203], v[28:31]\n\t" \
"ds_read_b64_tr_b16 v[130:131], v210 offset:40960\n\t" \
"v_mfma_f32_16x16x32_bf16 v[32:35], v[136:139], v[204:207], v[32:35]\n\t" \
"ds_read_b64_tr_b16 v[132:133], v211 offset:32768\n\t" \
"v_mfma_f32_16x16x32_bf16 v[36:39], v[140:143], v[180:183], v[36:39]\n\t" \
"ds_read_b64_tr_b16 v[134:135], v211 offset:40960\n\t" \
"v_mfma_f32_16x16x32_bf16 v[40:43], v[140:143], v[184:187], v[40:43]\n\t" \
"ds_read_b128 v[152:155], v220 offset:16384\n\t" \
"v_mfma_f32_16x16x32_bf16 v[44:47], v[140:143], v[188:191], v[44:47]\n\t" \
"ds_read_b128 v[156:159], v220 offset:17408\n\t" \
"v_mfma_f32_16x16x32_bf16 v[48:51], v[140:143], v[192:195], v[48:51]\n\t" \
"ds_read_b128 v[160:163], v220 offset:18432\n\t" \
"v_mfma_f32_16x16x32_bf16 v[52:55], v[140:143], v[196:199], v[52:55]\n\t" \
"ds_read_b128 v[164:167], v220 offset:19456\n\t" \
"v_mfma_f32_16x16x32_bf16 v[56:59], v[140:143], v[200:203], v[56:59]\n\t" \
"ds_read_b128 v[168:171], v220 offset:20480\n\t" \
"v_mfma_f32_16x16x32_bf16 v[60:63], v[140:143], v[204:207], v[60:63]\n\t" \
"ds_read_b128 v[172:175], v220 offset:21504\n\t" \
"v_mfma_f32_16x16x32_bf16 v[64:67], v[144:147], v[180:183], v[64:67]\n\t" \
"ds_read_b128 v[176:179], v220 offset:22528\n\t" \
"v_mfma_f32_16x16x32_bf16 v[68:71], v[144:147], v[184:187], v[68:71]\n\t" \
"v_mfma_f32_16x16x32_bf16 v[72:75], v[144:147], v[188:191], v[72:75]\n\t" \
"v_mov_b32 v242, 3\n\t" \
"v_mfma_f32_16x16x32_bf16 v[76:79], v[144:147], v[192:195], v[76:79]\n\t" \
"v_mfma_f32_16x16x32_bf16 v[80:83], v[144:147], v[196:199], v[80:83]\n\t" \
"v_mfma_f32_16x16x32_bf16 v[84:87], v[144:147], v[200:203], v[84:87]\n\t" \
"v_mfma_f32_16x16x32_bf16 v[88:91], v[144:147], v[204:207], v[88:91]\n\t" \
"s_waitcnt lgkmcnt(0)\n\t" \
"v_mfma_f32_16x16x32_bf16 v[92:95], v[148:151], v[180:183], v[92:95]\n\t" \
"ds_write_b32 v230, v242\n\t" \
"v_mfma_f32_16x16x32_bf16 v[96:99], v[148:151], v[184:187], v[96:99]\n\t" \
"v_mfma_f32_16x16x32_bf16 v[100:103], v[148:151], v[188:191], v[100:103]\n\t" \
"v_mfma_f32_16x16x32_bf16 v[104:107], v[148:151], v[192:195], v[104:107]\n\t" \
"v_mfma_f32_16x16x32_bf16 v[108:111], v[148:151], v[196:199], v[108:111]\n\t" \
"v_mfma_f32_16x16x32_bf16 v[112:115], v[148:151], v[200:203], v[112:115]\n\t" \
"v_mfma_f32_16x16x32_bf16 v[116:119], v[148:151], v[204:207], v[116:119]\n\t" \
"v_mfma_f32_16x16x32_bf16 v[8:11], v[120:123], v[152:155], v[8:11]\n\t" \
"ds_read_b64_tr_b16 v[136:137], v208 offset:49152\n\t" \
"v_mfma_f32_16x16x32_bf16 v[12:15], v[120:123], v[156:159], v[12:15]\n\t" \
"ds_read_b64_tr_b16 v[138:139], v208 offset:57344\n\t" \
"v_mfma_f32_16x16x32_bf16 v[16:19], v[120:123], v[160:163], v[16:19]\n\t" \
"ds_read_b64_tr_b16 v[140:141], v209 offset:49152\n\t" \
"v_mfma_f32_16x16x32_bf16 v[20:23], v[120:123], v[164:167], v[20:23]\n\t" \
"ds_read_b64_tr_b16 v[142:143], v209 offset:57344\n\t" \
"v_mfma_f32_16x16x32_bf16 v[24:27], v[120:123], v[168:171], v[24:27]\n\t" \
"ds_read_b64_tr_b16 v[144:145], v210 offset:49152\n\t" \
"v_mfma_f32_16x16x32_bf16 v[28:31], v[120:123], v[172:175], v[28:31]\n\t" \
"ds_read_b64_tr_b16 v[146:147], v210 offset:57344\n\t" \
"v_mfma_f32_16x16x32_bf16 v[32:35], v[120:123], v[176:179], v[32:35]\n\t" \
"ds_read_b64_tr_b16 v[148:149], v211 offset:49152\n\t" \
"v_mfma_f32_16x16x32_bf16 v[36:39], v[124:127], v[152:155], v[36:39]\n\t" \
"ds_read_b64_tr_b16 v[150:151], v211 offset:57344\n\t" \
"v_mfma_f32_16x16x32_bf16 v[40:43], v[124:127], v[156:159], v[40:43]\n\t" \
"ds_read_b128 v[180:183], v220 offset:24576\n\t" \
"v_mfma_f32_16x16x32_bf16 v[44:47], v[124:127], v[160:163], v[44:47]\n\t" \
"ds_read_b128 v[184:187], v220 offset:25600\n\t" \
"v_mfma_f32_16x16x32_bf16 v[48:51], v[124:127], v[164:167], v[48:51]\n\t" \
"ds_read_b128 v[188:191], v220 offset:26624\n\t" \
"v_mfma_f32_16x16x32_bf16 v[52:55], v[124:127], v[168:171], v[52:55]\n\t" \
"ds_read_b128 v[192:195], v220 offset:27648\n\t" \
"v_mfma_f32_16x16x32_bf16 v[56:59], v[124:127], v[172:175], v[56:59]\n\t" \
"ds_read_b128 v[196:199], v220 offset:28672\n\t" \
"v_mfma_f32_16x16x32_bf16 v[60:63], v[124:127], v[176:179], v[60:63]\n\t" \
"ds_read_b128 v[200:203], v220 offset:29696\n\t" \
"v_mfma_f32_16x16x32_bf16 v[64:67], v[128:131], v[152:155], v[64:67]\n\t" \
"ds_read_b128 v[204:207], v220 offset:30720\n\t" \
"v_mfma_f32_16x16x32_bf16 v[68:71], v[128:131], v[156:159], v[68:71]\n\t" \
"v_mfma_f32_16x16x32_bf16 v[72:75], v[128:131], v[160:163], v[72:75]\n\t" \
"v_mov_b32 v242, 4\n\t" \
"v_mfma_f32_16x16x32_bf16 v[76:79], v[128:131], v[164:167], v[76:79]\n\t" \
"v_mfma_f32_16x16x32_bf16 v[80:83], v[128:131], v[168:171], v[80:83]\n\t" \
"v_mfma_f32_16x16x32_bf16 v[84:87], v[128:131], v[172:175], v[84:87]\n\t" \
"v_mfma_f32_16x16x32_bf16 v[88:91], v[128:131], v[176:179], v[88:91]\n\t" \
"s_waitcnt lgkmcnt(0)\n\t" \
"v_mfma_f32_16x16x32_bf16 v[92:95], v[132:135], v[152:155], v[92:95]\n\t" \
"ds_write_b32 v230, v242\n\t" \
"v_mfma_f32_16x16x32_bf16 v[96:99], v[132:135], v[156:159], v[96:99]\n\t" \
"v_mfma_f32_16x16x32_bf16 v[100:103], v[132:135], v[160:163], v[100:103]\n\t" \
"v_mfma_f32_16x16x32_bf16 v[104:107], v[132:135], v[164:167], v[104:107]\n\t" \
"v_mfma_f32_16x16x32_bf16 v[108:111], v[132:135], v[168:171], v[108:111]\n\t" \
"v_mfma_f32_16x16x32_bf16 v[112:115], v[132:135], v[172:175], v[112:115]\n\t" \
"v_mfma_f32_16x16x32_bf16 v[116:119], v[132:135], v[176:179], v[116:119]\n\t" \
"s_cmp_eq_u32 s74, 1\n\t" \
"s_cbranch_scc0 L_r52_%=\n\t" \
"s_mov_b32 s3, 200000\n\t" \
"L_sl53_%=:\n\t" \
"ds_read_b128 v[246:249], v229 offset:32\n\t" \
"s_sub_u32 s3, s3, 1\n\t" \
"s_cbranch_scc1 L_sd53_%=\n\t" \
"s_waitcnt lgkmcnt(0)\n\t" \
"v_min3_u32 v246, v246, v247, v248\n\t" \
"v_min_u32 v246, v246, v249\n\t" \
"v_cmp_gt_u32 vcc, 3, v246\n\t" \
"s_cbranch_vccnz L_sl53_%=\n\t" \
"L_sd53_%=:\n\t" \
"s_waitcnt lgkmcnt(0)\n\t" \
"s_mov_b32 s60, s39\n\t" \
"s_add_u32 s60, s60, 4\n\t" \
"s_mul_i32 s61, s60, 28672\n\t" \
"s_add_u32 s61, s61, s82\n\t" \
"s_lshl_b32 s62, s60, 18\n\t" \
"s_add_u32 s61, s61, 21504\n\t" \
"s_add_u32 s62, s62, 0x30000\n\t" \
"s_add_u32 m0, s78, 0x8000\n\t" \
"s_nop 0\n\t" \
"buffer_load_dwordx4 v225, s[20:23], s62 offen offset:0 lds\n\t" \
"buffer_load_dwordx4 v226, s[20:23], s62 offen offset:1024 lds\n\t" \
"buffer_load_dwordx4 v227, s[20:23], s62 offen offset:2048 lds\n\t" \
"buffer_load_dwordx4 v228, s[20:23], s62 offen offset:3072 lds\n\t" \
"s_add_u32 m0, m0, 4096\n\t" \
"s_add_u32 s62, s62, 0x4000\n\t" \
"buffer_load_dwordx4 v225, s[20:23], s62 offen offset:0 lds\n\t" \
"buffer_load_dwordx4 v226, s[20:23], s62 offen offset:1024 lds\n\t" \
"buffer_load_dwordx4 v227, s[20:23], s62 offen offset:2048 lds\n\t" \
"buffer_load_dwordx4 v228, s[20:23], s62 offen offset:3072 lds\n\t" \
"s_waitcnt vmcnt(18)\n\t" \
"v_mov_b32 v242, 1\n\t" \
"ds_write_b32 v231, v242\n\t" \
"L_r52_%=:\n\t" \
"s_mov_b32 s3, 200000\n\t" \
"L_sl54_%=:\n\t" \
"ds_read_b128 v[246:249], v229 offset:48\n\t" \
"s_sub_u32 s3, s3, 1\n\t" \
"s_cbranch_scc1 L_sd54_%=\n\t" \
"s_waitcnt lgkmcnt(0)\n\t" \
"v_min3_u32 v246, v246, v247, v247\n\t" \
"v_cmp_gt_u32 vcc, 1, v246\n\t" \
"s_cbranch_vccnz L_sl54_%=\n\t" \
"L_sd54_%=:\n\t" \
"s_waitcnt lgkmcnt(0)\n\t" \
"s_mov_b32 s3, 200000\n\t" \
"L_sl55_%=:\n\t" \
"ds_read_b128 v[246:249], v229 offset:16\n\t" \
"s_sub_u32 s3, s3, 1\n\t" \
"s_cbranch_scc1 L_sd55_%=\n\t" \
"s_waitcnt lgkmcnt(0)\n\t" \
"v_min3_u32 v246, v246, v247, v248\n\t" \
"v_min_u32 v246, v246, v249\n\t" \
"v_cmp_gt_u32 vcc, 1, v246\n\t" \
"s_cbranch_vccnz L_sl55_%=\n\t" \
"L_sd55_%=:\n\t" \
"s_waitcnt lgkmcnt(0)\n\t" \
"s_setprio 1\n\t" \
"v_mfma_f32_16x16x32_bf16 v[8:11], v[136:139], v[180:183], v[8:11]\n\t" \
"ds_read_b64_tr_b16 v[120:121], v216 offset:16384\n\t" \
"v_mfma_f32_16x16x32_bf16 v[12:15], v[136:139], v[184:187], v[12:15]\n\t" \
"ds_read_b64_tr_b16 v[122:123], v216 offset:24576\n\t" \
"v_mfma_f32_16x16x32_bf16 v[16:19], v[136:139], v[188:191], v[16:19]\n\t" \
"ds_read_b64_tr_b16 v[124:125], v217 offset:16384\n\t" \
"v_mfma_f32_16x16x32_bf16 v[20:23], v[136:139], v[192:195], v[20:23]\n\t" \
"ds_read_b64_tr_b16 v[126:127], v217 offset:24576\n\t" \
"v_mfma_f32_16x16x32_bf16 v[24:27], v[136:139], v[196:199], v[24:27]\n\t" \
"ds_read_b64_tr_b16 v[128:129], v218 offset:16384\n\t" \
"v_mfma_f32_16x16x32_bf16 v[28:31], v[136:139], v[200:203], v[28:31]\n\t" \
"ds_read_b64_tr_b16 v[130:131], v218 offset:24576\n\t" \
"v_mfma_f32_16x16x32_bf16 v[32:35], v[136:139], v[204:207], v[32:35]\n\t" \
"ds_read_b64_tr_b16 v[132:133], v219 offset:16384\n\t" \
"v_mfma_f32_16x16x32_bf16 v[36:39], v[140:143], v[180:183], v[36:39]\n\t" \
"ds_read_b64_tr_b16 v[134:135], v219 offset:24576\n\t" \
"v_mfma_f32_16x16x32_bf16 v[40:43], v[140:143], v[184:187], v[40:43]\n\t" \
"ds_read_b128 v[152:155], v222 offset:0\n\t" \
"v_mfma_f32_16x16x32_bf16 v[44:47], v[140:143], v[188:191], v[44:47]\n\t" \
"ds_read_b128 v[156:159], v222 offset:1024\n\t" \
"v_mfma_f32_16x16x32_bf16 v[48:51], v[140:143], v[192:195], v[48:51]\n\t" \
"ds_read_b128 v[160:163], v222 offset:2048\n\t" \
"v_mfma_f32_16x16x32_bf16 v[52:55], v[140:143], v[196:199], v[52:55]\n\t" \
"ds_read_b128 v[164:167], v222 offset:3072\n\t" \
"v_mfma_f32_16x16x32_bf16 v[56:59], v[140:143], v[200:203], v[56:59]\n\t" \
"ds_read_b128 v[168:171], v222 offset:4096\n\t" \
"v_mfma_f32_16x16x32_bf16 v[60:63], v[140:143], v[204:207], v[60:63]\n\t" \
"ds_read_b128 v[172:175], v222 offset:5120\n\t" \
"v_mfma_f32_16x16x32_bf16 v[64:67], v[144:147], v[180:183], v[64:67]\n\t" \
"ds_read_b128 v[176:179], v222 offset:6144\n\t" \
"v_mfma_f32_16x16x32_bf16 v[68:71], v[144:147], v[184:187], v[68:71]\n\t" \
"v_mfma_f32_16x16x32_bf16 v[72:75], v[144:147], v[188:191], v[72:75]\n\t" \
"v_mov_b32 v242, 5\n\t" \
"v_mfma_f32_16x16x32_bf16 v[76:79], v[144:147], v[192:195], v[76:79]\n\t" \
"v_mfma_f32_16x16x32_bf16 v[80:83], v[144:147], v[196:199], v[80:83]\n\t" \
"v_mfma_f32_16x16x32_bf16 v[84:87], v[144:147], v[200:203], v[84:87]\n\t" \
"v_mfma_f32_16x16x32_bf16 v[88:91], v[144:147], v[204:207], v[88:91]\n\t" \
"s_waitcnt lgkmcnt(0)\n\t" \
"v_mfma_f32_16x16x32_bf16 v[92:95], v[148:151], v[180:183], v[92:95]\n\t" \
"ds_write_b32 v230, v242\n\t" \
"v_mfma_f32_16x16x32_bf16 v[96:99], v[148:151], v[184:187], v[96:99]\n\t" \
"v_mfma_f32_16x16x32_bf16 v[100:103], v[148:151], v[188:191], v[100:103]\n\t" \
"v_mfma_f32_16x16x32_bf16 v[104:107], v[148:151], v[192:195], v[104:107]\n\t" \
"v_mfma_f32_16x16x32_bf16 v[108:111], v[148:151], v[196:199], v[108:111]\n\t" \
"v_mfma_f32_16x16x32_bf16 v[112:115], v[148:151], v[200:203], v[112:115]\n\t" \
"v_mfma_f32_16x16x32_bf16 v[116:119], v[148:151], v[204:207], v[116:119]\n\t" \
"v_mfma_f32_16x16x32_bf16 v[8:11], v[120:123], v[152:155], v[8:11]\n\t" \
"ds_read_b64_tr_b16 v[136:137], v212 offset:49152\n\t" \
"v_mfma_f32_16x16x32_bf16 v[12:15], v[120:123], v[156:159], v[12:15]\n\t" \
"ds_read_b64_tr_b16 v[138:139], v212 offset:57344\n\t" \
"v_mfma_f32_16x16x32_bf16 v[16:19], v[120:123], v[160:163], v[16:19]\n\t" \
"ds_read_b64_tr_b16 v[140:141], v213 offset:49152\n\t" \
"v_mfma_f32_16x16x32_bf16 v[20:23], v[120:123], v[164:167], v[20:23]\n\t" \
"ds_read_b64_tr_b16 v[142:143], v213 offset:57344\n\t" \
"v_mfma_f32_16x16x32_bf16 v[24:27], v[120:123], v[168:171], v[24:27]\n\t" \
"ds_read_b64_tr_b16 v[144:145], v214 offset:49152\n\t" \
"v_mfma_f32_16x16x32_bf16 v[28:31], v[120:123], v[172:175], v[28:31]\n\t" \
"ds_read_b64_tr_b16 v[146:147], v214 offset:57344\n\t" \
"v_mfma_f32_16x16x32_bf16 v[32:35], v[120:123], v[176:179], v[32:35]\n\t" \
"ds_read_b64_tr_b16 v[148:149], v215 offset:49152\n\t" \
"v_mfma_f32_16x16x32_bf16 v[36:39], v[124:127], v[152:155], v[36:39]\n\t" \
"ds_read_b64_tr_b16 v[150:151], v215 offset:57344\n\t" \
"v_mfma_f32_16x16x32_bf16 v[40:43], v[124:127], v[156:159], v[40:43]\n\t" \
"ds_read_b128 v[180:183], v222 offset:8192\n\t" \
"v_mfma_f32_16x16x32_bf16 v[44:47], v[124:127], v[160:163], v[44:47]\n\t" \
"ds_read_b128 v[184:187], v222 offset:9216\n\t" \
"v_mfma_f32_16x16x32_bf16 v[48:51], v[124:127], v[164:167], v[48:51]\n\t" \
"ds_read_b128 v[188:191], v222 offset:10240\n\t" \
"v_mfma_f32_16x16x32_bf16 v[52:55], v[124:127], v[168:171], v[52:55]\n\t" \
"ds_read_b128 v[192:195], v222 offset:11264\n\t" \
"v_mfma_f32_16x16x32_bf16 v[56:59], v[124:127], v[172:175], v[56:59]\n\t" \
"ds_read_b128 v[196:199], v222 offset:12288\n\t" \
"v_mfma_f32_16x16x32_bf16 v[60:63], v[124:127], v[176:179], v[60:63]\n\t" \
"ds_read_b128 v[200:203], v222 offset:13312\n\t" \
"v_mfma_f32_16x16x32_bf16 v[64:67], v[128:131], v[152:155], v[64:67]\n\t" \
"ds_read_b128 v[204:207], v222 offset:14336\n\t" \
"v_mfma_f32_16x16x32_bf16 v[68:71], v[128:131], v[156:159], v[68:71]\n\t" \
"v_mfma_f32_16x16x32_bf16 v[72:75], v[128:131], v[160:163], v[72:75]\n\t" \
"v_mov_b32 v242, 6\n\t" \
"v_mfma_f32_16x16x32_bf16 v[76:79], v[128:131], v[164:167], v[76:79]\n\t" \
"v_mfma_f32_16x16x32_bf16 v[80:83], v[128:131], v[168:171], v[80:83]\n\t" \
"v_mfma_f32_16x16x32_bf16 v[84:87], v[128:131], v[172:175], v[84:87]\n\t" \
"v_mfma_f32_16x16x32_bf16 v[88:91], v[128:131], v[176:179], v[88:91]\n\t" \
"s_waitcnt lgkmcnt(0)\n\t" \
"v_mfma_f32_16x16x32_bf16 v[92:95], v[132:135], v[152:155], v[92:95]\n\t" \
"ds_write_b32 v230, v242\n\t" \
"v_mfma_f32_16x16x32_bf16 v[96:99], v[132:135], v[156:159], v[96:99]\n\t" \
"v_mfma_f32_16x16x32_bf16 v[100:103], v[132:135], v[160:163], v[100:103]\n\t" \
"v_mfma_f32_16x16x32_bf16 v[104:107], v[132:135], v[164:167], v[104:107]\n\t" \
"v_mfma_f32_16x16x32_bf16 v[108:111], v[132:135], v[168:171], v[108:111]\n\t" \
"v_mfma_f32_16x16x32_bf16 v[112:115], v[132:135], v[172:175], v[112:115]\n\t" \
"v_mfma_f32_16x16x32_bf16 v[116:119], v[132:135], v[176:179], v[116:119]\n\t" \
"s_cmp_eq_u32 s74, 1\n\t" \
"s_cbranch_scc0 L_r56_%=\n\t" \
"s_waitcnt vmcnt(0)\n\t" \
"v_mov_b32 v242, 2\n\t" \
"ds_write_b32 v231, v242\n\t" \
"L_r56_%=:\n\t" \
"s_mov_b32 s3, 200000\n\t" \
"L_sl57_%=:\n\t" \
"ds_read_b128 v[246:249], v229 offset:48\n\t" \
"s_sub_u32 s3, s3, 1\n\t" \
"s_cbranch_scc1 L_sd57_%=\n\t" \
"s_waitcnt lgkmcnt(0)\n\t" \
"v_min3_u32 v246, v246, v247, v247\n\t" \
"v_cmp_gt_u32 vcc, 2, v246\n\t" \
"s_cbranch_vccnz L_sl57_%=\n\t" \
"L_sd57_%=:\n\t" \
"s_waitcnt lgkmcnt(0)\n\t" \
"s_mov_b32 s3, 200000\n\t" \
"L_sl58_%=:\n\t" \
"ds_read_b128 v[246:249], v229 offset:16\n\t" \
"s_sub_u32 s3, s3, 1\n\t" \
"s_cbranch_scc1 L_sd58_%=\n\t" \
"s_waitcnt lgkmcnt(0)\n\t" \
"v_min3_u32 v246, v246, v247, v248\n\t" \
"v_min_u32 v246, v246, v249\n\t" \
"v_cmp_gt_u32 vcc, 2, v246\n\t" \
"s_cbranch_vccnz L_sl58_%=\n\t" \
"L_sd58_%=:\n\t" \
"s_waitcnt lgkmcnt(0)\n\t" \
"s_mov_b32 s70, 6\n\t" \
"s_mov_b32 s65, 0x57932\n\t" \
"s_mov_b32 s66, 0x68104\n\t" \
"v_add_u32 v212, 0x14000, v208\n\t" \
"v_add_u32 v213, 0x14000, v209\n\t" \
"v_add_u32 v214, 0x14000, v210\n\t" \
"v_add_u32 v215, 0x14000, v211\n\t" \
"v_add_u32 v221, 0x18000, v220\n\t" \
"L_tl_%=:\n\t" \
"v_mfma_f32_16x16x32_bf16 v[8:11], v[136:139], v[180:183], v[8:11]\n\t" \
"ds_read_b64_tr_b16 v[120:121], v212 offset:0\n\t" \
"v_mfma_f32_16x16x32_bf16 v[12:15], v[136:139], v[184:187], v[12:15]\n\t" \
"ds_read_b64_tr_b16 v[122:123], v212 offset:8192\n\t" \
"v_mfma_f32_16x16x32_bf16 v[16:19], v[136:139], v[188:191], v[16:19]\n\t" \
"ds_read_b64_tr_b16 v[124:125], v213 offset:0\n\t" \
"s_add_u32 s68, s70, 1\n\t" \
"v_mfma_f32_16x16x32_bf16 v[20:23], v[136:139], v[192:195], v[20:23]\n\t" \
"ds_read_b64_tr_b16 v[126:127], v213 offset:8192\n\t" \
"s_and_b32 s67, s65, 15\n\t" \
"s_lshr_b32 s65, s65, 4\n\t" \
"s_lshl_b32 s67, s67, 16\n\t" \
"v_mfma_f32_16x16x32_bf16 v[24:27], v[136:139], v[196:199], v[24:27]\n\t" \
"ds_read_b64_tr_b16 v[128:129], v214 offset:0\n\t" \
"s_or_b32 s65, s65, s67\n\t" \
"s_lshr_b32 s67, s67, 2\n\t" \
"v_mfma_f32_16x16x32_bf16 v[28:31], v[136:139], v[200:203], v[28:31]\n\t" \
"ds_read_b64_tr_b16 v[130:131], v214 offset:8192\n\t" \
"v_mfma_f32_16x16x32_bf16 v[32:35], v[136:139], v[204:207], v[32:35]\n\t" \
"ds_read_b64_tr_b16 v[132:133], v215 offset:0\n\t" \
"v_mfma_f32_16x16x32_bf16 v[36:39], v[140:143], v[180:183], v[36:39]\n\t" \
"ds_read_b64_tr_b16 v[134:135], v215 offset:8192\n\t" \
"v_mfma_f32_16x16x32_bf16 v[40:43], v[140:143], v[184:187], v[40:43]\n\t" \
"ds_read_b128 v[152:155], v221 offset:0\n\t" \
"v_mfma_f32_16x16x32_bf16 v[44:47], v[140:143], v[188:191], v[44:47]\n\t" \
"ds_read_b128 v[156:159], v221 offset:1024\n\t" \
"v_mfma_f32_16x16x32_bf16 v[48:51], v[140:143], v[192:195], v[48:51]\n\t" \
"ds_read_b128 v[160:163], v221 offset:2048\n\t" \
"v_mfma_f32_16x16x32_bf16 v[52:55], v[140:143], v[196:199], v[52:55]\n\t" \
"ds_read_b128 v[164:167], v221 offset:3072\n\t" \
"v_mfma_f32_16x16x32_bf16 v[56:59], v[140:143], v[200:203], v[56:59]\n\t" \
"ds_read_b128 v[168:171], v221 offset:4096\n\t" \
"v_mfma_f32_16x16x32_bf16 v[60:63], v[140:143], v[204:207], v[60:63]\n\t" \
"ds_read_b128 v[172:175], v221 offset:5120\n\t" \
"v_mfma_f32_16x16x32_bf16 v[64:67], v[144:147], v[180:183], v[64:67]\n\t" \
"ds_read_b128 v[176:179], v221 offset:6144\n\t" \
"v_mfma_f32_16x16x32_bf16 v[68:71], v[144:147], v[184:187], v[68:71]\n\t" \
"ds_read_b128 v[246:249], v229\n\t" \
"v_mfma_f32_16x16x32_bf16 v[72:75], v[144:147], v[188:191], v[72:75]\n\t" \
"v_mov_b32 v242, s68\n\t" \
"v_mfma_f32_16x16x32_bf16 v[76:79], v[144:147], v[192:195], v[76:79]\n\t" \
"s_add_u32 s68, s70, 2\n\t" \
"s_cmp_eq_u32 s68, 33\n\t" \
"v_mfma_f32_16x16x32_bf16 v[80:83], v[144:147], v[196:199], v[80:83]\n\t" \
"s_cselect_b32 s68, 31, s68\n\t" \
"v_mfma_f32_16x16x32_bf16 v[84:87], v[144:147], v[200:203], v[84:87]\n\t" \
"v_mfma_f32_16x16x32_bf16 v[88:91], v[144:147], v[204:207], v[88:91]\n\t" \
"s_waitcnt lgkmcnt(0)\n\t" \
"v_mfma_f32_16x16x32_bf16 v[92:95], v[148:151], v[180:183], v[92:95]\n\t" \
"ds_write_b32 v230, v242\n\t" \
"v_mfma_f32_16x16x32_bf16 v[96:99], v[148:151], v[184:187], v[96:99]\n\t" \
"v_add_u32 v212, s67, v208\n\t" \
"v_add_u32 v213, s67, v209\n\t" \
"v_mfma_f32_16x16x32_bf16 v[100:103], v[148:151], v[188:191], v[100:103]\n\t" \
"v_add_u32 v214, s67, v210\n\t" \
"v_add_u32 v215, s67, v211\n\t" \
"v_mfma_f32_16x16x32_bf16 v[104:107], v[148:151], v[192:195], v[104:107]\n\t" \
"v_min_u32 v246, v247, v249\n\t" \
"v_cmp_gt_u32 vcc, s68, v246\n\t" \
"v_mfma_f32_16x16x32_bf16 v[108:111], v[148:151], v[196:199], v[108:111]\n\t" \
"v_mfma_f32_16x16x32_bf16 v[112:115], v[148:151], v[200:203], v[112:115]\n\t" \
"v_mfma_f32_16x16x32_bf16 v[116:119], v[148:151], v[204:207], v[116:119]\n\t" \
"s_cbranch_vccnz L_os59_%=\n\t" \
"L_ob59_%=:\n\t" \
"s_add_u32 s70, s70, 1\n\t" \
"v_mfma_f32_16x16x32_bf16 v[8:11], v[120:123], v[152:155], v[8:11]\n\t" \
"ds_read_b64_tr_b16 v[136:137], v212 offset:0\n\t" \
"v_mfma_f32_16x16x32_bf16 v[12:15], v[120:123], v[156:159], v[12:15]\n\t" \
"ds_read_b64_tr_b16 v[138:139], v212 offset:8192\n\t" \
"v_mfma_f32_16x16x32_bf16 v[16:19], v[120:123], v[160:163], v[16:19]\n\t" \
"ds_read_b64_tr_b16 v[140:141], v213 offset:0\n\t" \
"s_add_u32 s68, s70, 1\n\t" \
"v_mfma_f32_16x16x32_bf16 v[20:23], v[120:123], v[164:167], v[20:23]\n\t" \
"ds_read_b64_tr_b16 v[142:143], v213 offset:8192\n\t" \
"s_and_b32 s67, s65, 15\n\t" \
"s_lshr_b32 s65, s65, 4\n\t" \
"s_lshl_b32 s67, s67, 16\n\t" \
"v_mfma_f32_16x16x32_bf16 v[24:27], v[120:123], v[168:171], v[24:27]\n\t" \
"ds_read_b64_tr_b16 v[144:145], v214 offset:0\n\t" \
"s_or_b32 s65, s65, s67\n\t" \
"s_lshr_b32 s67, s67, 2\n\t" \
"v_mfma_f32_16x16x32_bf16 v[28:31], v[120:123], v[172:175], v[28:31]\n\t" \
"ds_read_b64_tr_b16 v[146:147], v214 offset:8192\n\t" \
"s_and_b32 s79, s66, 15\n\t" \
"s_lshr_b32 s66, s66, 4\n\t" \
"s_lshl_b32 s79, s79, 16\n\t" \
"v_mfma_f32_16x16x32_bf16 v[32:35], v[120:123], v[176:179], v[32:35]\n\t" \
"ds_read_b64_tr_b16 v[148:149], v215 offset:0\n\t" \
"s_or_b32 s66, s66, s79\n\t" \
"s_lshr_b32 s79, s79, 2\n\t" \
"v_mfma_f32_16x16x32_bf16 v[36:39], v[124:127], v[152:155], v[36:39]\n\t" \
"ds_read_b64_tr_b16 v[150:151], v215 offset:8192\n\t" \
"v_mfma_f32_16x16x32_bf16 v[40:43], v[124:127], v[156:159], v[40:43]\n\t" \
"ds_read_b128 v[180:183], v221 offset:8192\n\t" \
"v_mfma_f32_16x16x32_bf16 v[44:47], v[124:127], v[160:163], v[44:47]\n\t" \
"ds_read_b128 v[184:187], v221 offset:9216\n\t" \
"v_mfma_f32_16x16x32_bf16 v[48:51], v[124:127], v[164:167], v[48:51]\n\t" \
"ds_read_b128 v[188:191], v221 offset:10240\n\t" \
"v_mfma_f32_16x16x32_bf16 v[52:55], v[124:127], v[168:171], v[52:55]\n\t" \
"ds_read_b128 v[192:195], v221 offset:11264\n\t" \
"v_mfma_f32_16x16x32_bf16 v[56:59], v[124:127], v[172:175], v[56:59]\n\t" \
"ds_read_b128 v[196:199], v221 offset:12288\n\t" \
"v_mfma_f32_16x16x32_bf16 v[60:63], v[124:127], v[176:179], v[60:63]\n\t" \
"ds_read_b128 v[200:203], v221 offset:13312\n\t" \
"v_mfma_f32_16x16x32_bf16 v[64:67], v[128:131], v[152:155], v[64:67]\n\t" \
"ds_read_b128 v[204:207], v221 offset:14336\n\t" \
"v_mfma_f32_16x16x32_bf16 v[68:71], v[128:131], v[156:159], v[68:71]\n\t" \
"ds_read_b128 v[246:249], v229\n\t" \
"v_mfma_f32_16x16x32_bf16 v[72:75], v[128:131], v[160:163], v[72:75]\n\t" \
"v_mov_b32 v242, s68\n\t" \
"v_mfma_f32_16x16x32_bf16 v[76:79], v[128:131], v[164:167], v[76:79]\n\t" \
"s_add_u32 s68, s70, 2\n\t" \
"s_cmp_eq_u32 s68, 33\n\t" \
"v_mfma_f32_16x16x32_bf16 v[80:83], v[128:131], v[168:171], v[80:83]\n\t" \
"s_cselect_b32 s68, 31, s68\n\t" \
"v_mfma_f32_16x16x32_bf16 v[84:87], v[128:131], v[172:175], v[84:87]\n\t" \
"v_add_u32 v221, s79, v220\n\t" \
"v_mfma_f32_16x16x32_bf16 v[88:91], v[128:131], v[176:179], v[88:91]\n\t" \
"s_waitcnt lgkmcnt(0)\n\t" \
"v_mfma_f32_16x16x32_bf16 v[92:95], v[132:135], v[152:155], v[92:95]\n\t" \
"ds_write_b32 v230, v242\n\t" \
"v_mfma_f32_16x16x32_bf16 v[96:99], v[132:135], v[156:159], v[96:99]\n\t" \
"v_add_u32 v212, s67, v208\n\t" \
"v_add_u32 v213, s67, v209\n\t" \
"v_mfma_f32_16x16x32_bf16 v[100:103], v[132:135], v[160:163], v[100:103]\n\t" \
"v_add_u32 v214, s67, v210\n\t" \
"v_add_u32 v215, s67, v211\n\t" \
"v_mfma_f32_16x16x32_bf16 v[104:107], v[132:135], v[164:167], v[104:107]\n\t" \
"v_min_u32 v246, v246, v248\n\t" \
"v_cmp_gt_u32 vcc, s68, v246\n\t" \
"v_mfma_f32_16x16x32_bf16 v[108:111], v[132:135], v[168:171], v[108:111]\n\t" \
"v_mfma_f32_16x16x32_bf16 v[112:115], v[132:135], v[172:175], v[112:115]\n\t" \
"v_mfma_f32_16x16x32_bf16 v[116:119], v[132:135], v[176:179], v[116:119]\n\t" \
"s_cbranch_vccnz L_os60_%=\n\t" \
"L_ob60_%=:\n\t" \
"s_add_u32 s70, s70, 1\n\t" \
"s_cmp_lt_u32 s70, 32\n\t" \
"s_cbranch_scc1 L_tl_%=\n\t" \
"s_lshl_b32 s70, s38, 3\n\t" \
"s_add_u32 s70, s70, s35\n\t" \
"s_lshl_b32 s70, s70, 2\n\t" \
"s_add_u32 s70, s70, s37\n\t" \
"s_lshl_b32 s70, s70, 18\n\t" \
"s_lshl_b32 s60, s32, 12\n\t" \
"s_add_u32 s70, s70, s60\n\t" \
"s_add_u32 s70, s70, 0x1000000\n\t" \
"v_lshlrev_b32 v220, 3, v0\n\t" \
"s_add_u32 s60, s36, 1\n\t" \
"s_and_b32 s60, s60, 3\n\t" \
"s_lshl_b32 s62, s60, 16\n\t" \
"s_lshl_b32 s61, s36, 14\n\t" \
"s_add_u32 s62, s62, s61\n\t" \
"s_add_u32 s41, s62, s70\n\t" \
"s_lshl_b32 s62, s60, 14\n\t" \
"s_lshl_b32 s61, s36, 16\n\t" \
"s_add_u32 s62, s62, s61\n\t" \
"s_add_u32 s45, s62, s70\n\t" \
"s_add_u32 s60, s36, 2\n\t" \
"s_and_b32 s60, s60, 3\n\t" \
"s_lshl_b32 s62, s60, 16\n\t" \
"s_lshl_b32 s61, s36, 14\n\t" \
"s_add_u32 s62, s62, s61\n\t" \
"s_add_u32 s42, s62, s70\n\t" \
"s_lshl_b32 s62, s60, 14\n\t" \
"s_lshl_b32 s61, s36, 16\n\t" \
"s_add_u32 s62, s62, s61\n\t" \
"s_add_u32 s46, s62, s70\n\t" \
"s_add_u32 s60, s36, 3\n\t" \
"s_and_b32 s60, s60, 3\n\t" \
"s_lshl_b32 s62, s60, 16\n\t" \
"s_lshl_b32 s61, s36, 14\n\t" \
"s_add_u32 s62, s62, s61\n\t" \
"s_add_u32 s43, s62, s70\n\t" \
"s_lshl_b32 s62, s60, 14\n\t" \
"s_lshl_b32 s61, s36, 16\n\t" \
"s_add_u32 s62, s62, s61\n\t" \
"s_add_u32 s47, s62, s70\n\t" \
"v_mfma_f32_16x16x32_bf16 v[36:39], v[140:143], v[180:183], v[36:39]\n\t" \
"v_mfma_f32_16x16x32_bf16 v[40:43], v[140:143], v[184:187], v[40:43]\n\t" \
"v_mfma_f32_16x16x32_bf16 v[44:47], v[140:143], v[188:191], v[44:47]\n\t" \
"v_mfma_f32_16x16x32_bf16 v[48:51], v[140:143], v[192:195], v[48:51]\n\t" \
"s_nop 4\n\t" \
"v_cvt_pk_bf16_f32 v36, v36, v37\n\t" \
"v_cvt_pk_bf16_f32 v37, v38, v39\n\t" \
"buffer_store_dwordx2 v[36:37], v220, s[24:27], s41 offen offset:0 sc1\n\t" \
"v_mfma_f32_16x16x32_bf16 v[52:55], v[140:143], v[196:199], v[52:55]\n\t" \
"v_cvt_pk_bf16_f32 v40, v40, v41\n\t" \
"v_cvt_pk_bf16_f32 v41, v42, v43\n\t" \
"buffer_store_dwordx2 v[40:41], v220, s[24:27], s41 offen offset:512 sc1\n\t" \
"v_mfma_f32_16x16x32_bf16 v[56:59], v[140:143], v[200:203], v[56:59]\n\t" \
"v_cvt_pk_bf16_f32 v44, v44, v45\n\t" \
"v_cvt_pk_bf16_f32 v45, v46, v47\n\t" \
"buffer_store_dwordx2 v[44:45], v220, s[24:27], s41 offen offset:1024 sc1\n\t" \
"v_mfma_f32_16x16x32_bf16 v[60:63], v[140:143], v[204:207], v[60:63]\n\t" \
"v_cvt_pk_bf16_f32 v48, v48, v49\n\t" \
"v_cvt_pk_bf16_f32 v49, v50, v51\n\t" \
"buffer_store_dwordx2 v[48:49], v220, s[24:27], s41 offen offset:1536 sc1\n\t" \
"v_mfma_f32_16x16x32_bf16 v[64:67], v[144:147], v[180:183], v[64:67]\n\t" \
"v_cvt_pk_bf16_f32 v52, v52, v53\n\t" \
"v_cvt_pk_bf16_f32 v53, v54, v55\n\t" \
"buffer_store_dwordx2 v[52:53], v220, s[24:27], s41 offen offset:2048 sc1\n\t" \
"v_mfma_f32_16x16x32_bf16 v[68:71], v[144:147], v[184:187], v[68:71]\n\t" \
"v_cvt_pk_bf16_f32 v56, v56, v57\n\t" \
"v_cvt_pk_bf16_f32 v57, v58, v59\n\t" \
"buffer_store_dwordx2 v[56:57], v220, s[24:27], s41 offen offset:2560 sc1\n\t" \
"v_mfma_f32_16x16x32_bf16 v[72:75], v[144:147], v[188:191], v[72:75]\n\t" \
"v_cvt_pk_bf16_f32 v60, v60, v61\n\t" \
"v_cvt_pk_bf16_f32 v61, v62, v63\n\t" \
"buffer_store_dwordx2 v[60:61], v220, s[24:27], s41 offen offset:3072 sc1\n\t" \
"v_mfma_f32_16x16x32_bf16 v[76:79], v[144:147], v[192:195], v[76:79]\n\t" \
"v_cvt_pk_bf16_f32 v64, v64, v65\n\t" \
"v_cvt_pk_bf16_f32 v65, v66, v67\n\t" \
"buffer_store_dwordx2 v[64:65], v220, s[24:27], s42 offen offset:0 sc1\n\t" \
"v_mfma_f32_16x16x32_bf16 v[80:83], v[144:147], v[196:199], v[80:83]\n\t" \
"v_cvt_pk_bf16_f32 v68, v68, v69\n\t" \
"v_cvt_pk_bf16_f32 v69, v70, v71\n\t" \
"buffer_store_dwordx2 v[68:69], v220, s[24:27], s42 offen offset:512 sc1\n\t" \
"v_mfma_f32_16x16x32_bf16 v[84:87], v[144:147], v[200:203], v[84:87]\n\t" \
"v_cvt_pk_bf16_f32 v72, v72, v73\n\t" \
"v_cvt_pk_bf16_f32 v73, v74, v75\n\t" \
"buffer_store_dwordx2 v[72:73], v220, s[24:27], s42 offen offset:1024 sc1\n\t" \
"v_mfma_f32_16x16x32_bf16 v[88:91], v[144:147], v[204:207], v[88:91]\n\t" \
"v_cvt_pk_bf16_f32 v76, v76, v77\n\t" \
"v_cvt_pk_bf16_f32 v77, v78, v79\n\t" \
"buffer_store_dwordx2 v[76:77], v220, s[24:27], s42 offen offset:1536 sc1\n\t" \
"v_mfma_f32_16x16x32_bf16 v[92:95], v[148:151], v[180:183], v[92:95]\n\t" \
"v_cvt_pk_bf16_f32 v80, v80, v81\n\t" \
"v_cvt_pk_bf16_f32 v81, v82, v83\n\t" \
"buffer_store_dwordx2 v[80:81], v220, s[24:27], s42 offen offset:2048 sc1\n\t" \
"v_mfma_f32_16x16x32_bf16 v[96:99], v[148:151], v[184:187], v[96:99]\n\t" \
"v_cvt_pk_bf16_f32 v84, v84, v85\n\t" \
"v_cvt_pk_bf16_f32 v85, v86, v87\n\t" \
"buffer_store_dwordx2 v[84:85], v220, s[24:27], s42 offen offset:2560 sc1\n\t" \
"v_mfma_f32_16x16x32_bf16 v[100:103], v[148:151], v[188:191], v[100:103]\n\t" \
"v_cvt_pk_bf16_f32 v88, v88, v89\n\t" \
"v_cvt_pk_bf16_f32 v89, v90, v91\n\t" \
"buffer_store_dwordx2 v[88:89], v220, s[24:27], s42 offen offset:3072 sc1\n\t" \
"v_mfma_f32_16x16x32_bf16 v[104:107], v[148:151], v[192:195], v[104:107]\n\t" \
"v_cvt_pk_bf16_f32 v92, v92, v93\n\t" \
"v_cvt_pk_bf16_f32 v93, v94, v95\n\t" \
"buffer_store_dwordx2 v[92:93], v220, s[24:27], s43 offen offset:0 sc1\n\t" \
"v_mfma_f32_16x16x32_bf16 v[108:111], v[148:151], v[196:199], v[108:111]\n\t" \
"v_cvt_pk_bf16_f32 v96, v96, v97\n\t" \
"v_cvt_pk_bf16_f32 v97, v98, v99\n\t" \
"buffer_store_dwordx2 v[96:97], v220, s[24:27], s43 offen offset:512 sc1\n\t" \
"v_mfma_f32_16x16x32_bf16 v[112:115], v[148:151], v[200:203], v[112:115]\n\t" \
"v_cvt_pk_bf16_f32 v100, v100, v101\n\t" \
"v_cvt_pk_bf16_f32 v101, v102, v103\n\t" \
"buffer_store_dwordx2 v[100:101], v220, s[24:27], s43 offen offset:1024 sc1\n\t" \
"v_mfma_f32_16x16x32_bf16 v[116:119], v[148:151], v[204:207], v[116:119]\n\t" \
"v_cvt_pk_bf16_f32 v104, v104, v105\n\t" \
"v_cvt_pk_bf16_f32 v105, v106, v107\n\t" \
"buffer_store_dwordx2 v[104:105], v220, s[24:27], s43 offen offset:1536 sc1\n\t" \
"v_mfma_f32_16x16x32_bf16 v[8:11], v[136:139], v[180:183], v[8:11]\n\t" \
"v_cvt_pk_bf16_f32 v108, v108, v109\n\t" \
"v_cvt_pk_bf16_f32 v109, v110, v111\n\t" \
"buffer_store_dwordx2 v[108:109], v220, s[24:27], s43 offen offset:2048 sc1\n\t" \
"v_mfma_f32_16x16x32_bf16 v[12:15], v[136:139], v[184:187], v[12:15]\n\t" \
"v_cvt_pk_bf16_f32 v112, v112, v113\n\t" \
"v_cvt_pk_bf16_f32 v113, v114, v115\n\t" \
"buffer_store_dwordx2 v[112:113], v220, s[24:27], s43 offen offset:2560 sc1\n\t" \
"v_mfma_f32_16x16x32_bf16 v[16:19], v[136:139], v[188:191], v[16:19]\n\t" \
"v_cvt_pk_bf16_f32 v116, v116, v117\n\t" \
"v_cvt_pk_bf16_f32 v117, v118, v119\n\t" \
"buffer_store_dwordx2 v[116:117], v220, s[24:27], s43 offen offset:3072 sc1\n\t" \
"v_mfma_f32_16x16x32_bf16 v[20:23], v[136:139], v[192:195], v[20:23]\n\t" \
"v_mfma_f32_16x16x32_bf16 v[24:27], v[136:139], v[196:199], v[24:27]\n\t" \
"v_mfma_f32_16x16x32_bf16 v[28:31], v[136:139], v[200:203], v[28:31]\n\t" \
"v_mfma_f32_16x16x32_bf16 v[32:35], v[136:139], v[204:207], v[32:35]\n\t" \
"s_lshl_b32 s60, s32, 3\n\t" \
"s_add_u32 s61, s60, s83\n\t" \
"v_lshlrev_b32 v5, 5, v0\n\t" \
"v_mov_b32 v6, s50\n\t" \
"v_mov_b32 v7, s51\n\t" \
"s_lshl_b32 s62, 1, s36\n\t" \
"s_xor_b32 s62, s62, 15\n\t" \
"s_mov_b32 s63, 0\n\t" \
"s_lshl_b32 s64, s36, 5\n\t" \
"s_add_u32 s64, s64, s60\n\t" \
"s_add_u32 s64, s64, 128\n\t" \
"v_add_u32 v4, s64, v254\n\t" \
"v_and_b32 v5, 0x7f, v5\n\t" \
"v_and_b32 v1, 1, v0\n\t" \
"v_lshlrev_b32 v1, 4, v1\n\t" \
"v_mov_b32 v2, 0x3f80\n\t" \
"v_lshlrev_b32 v1, v1, v2\n\t" \
"v_bfe_u32 v2, v0, 1, 3\n\t" \
"v_lshrrev_b32 v3, 4, v0\n\t" \
"v_lshlrev_b32 v3, 1, v3\n\t" \
"v_cmp_eq_u32 vcc, v2, v3\n\t" \
"s_nop 1\n\t" \
"v_cndmask_b32 v208, 0, v1, vcc\n\t" \
"v_add_u32 v3, 1, v3\n\t" \
"v_cmp_eq_u32 vcc, v2, v3\n\t" \
"s_nop 1\n\t" \
"v_cndmask_b32 v209, 0, v1, vcc\n\t" \
"v_mov_b32 v210, v208\n\t" \
"v_mov_b32 v211, v209\n\t" \
"v_mov_b32 v126, 0\n\t" \
"v_mov_b32 v127, 0\n\t" \
"v_mov_b32 v134, 0\n\t" \
"v_mov_b32 v135, 0\n\t" \
"v_mov_b32 v142, 0\n\t" \
"v_mov_b32 v143, 0\n\t" \
"v_mov_b32 v150, 0\n\t" \
"v_mov_b32 v151, 0\n\t" \
"v_mov_b32 v158, 0\n\t" \
"v_mov_b32 v159, 0\n\t" \
"v_mov_b32 v166, 0\n\t" \
"v_mov_b32 v167, 0\n\t" \
"v_mov_b32 v174, 0\n\t" \
"v_mov_b32 v175, 0\n\t" \
"s_mul_i32 s70, s35, 458752\n\t" \
"s_lshl_b32 s60, s38, 11\n\t" \
"s_add_u32 s70, s70, s60\n\t" \
"s_lshl_b32 s60, s37, 9\n\t" \
"s_add_u32 s70, s70, s60\n\t" \
"s_lshl_b32 s60, s36, 7\n\t" \
"s_add_u32 s70, s70, s60\n\t" \
"s_lshl_b32 s60, s32, 5\n\t" \
"s_add_u32 s70, s70, s60\n\t" \
"v_and_b32 v1, 15, v0\n\t" \
"v_lshrrev_b32 v2, 4, v0\n\t" \
"v_lshlrev_b32 v1, 12, v1\n\t" \
"v_lshl_add_u32 v1, v2, 3, v1\n\t" \
"s_waitcnt vmcnt(0)\n\t" \
"s_mov_b64 exec, s[62:63]\n\t" \
"buffer_store_dwordx2 v[6:7], v5, s[24:27], s61 offen offset:128 sc1\n\t" \
"s_lshl_b32 s62, s62, 16\n\t" \
"s_mov_b64 exec, s[62:63]\n\t" \
"s_mov_b32 s3, 200000\n\t" \
"L_pl61_%=:\n\t" \
"buffer_load_dwordx2 v[2:3], v4, s[24:27], 0 offen sc1\n\t" \
"s_sub_u32 s3, s3, 1\n\t" \
"s_cbranch_scc1 L_pd61_%=\n\t" \
"s_waitcnt vmcnt(0)\n\t" \
"L_pc61_%=:\n\t" \
"v_cmp_ne_u64 vcc, v[2:3], v[252:253]\n\t" \
"s_cbranch_vccnz L_pl61_%=\n\t" \
"L_pd61_%=:\n\t" \
"s_waitcnt vmcnt(0)\n\t" \
"s_mov_b64 exec, -1\n\t" \
"buffer_load_dwordx2 v[120:121], v220, s[24:27], s45 offen offset:0 sc1\n\t" \
"buffer_load_dwordx2 v[122:123], v220, s[24:27], s46 offen offset:0 sc1\n\t" \
"buffer_load_dwordx2 v[124:125], v220, s[24:27], s47 offen offset:0 sc1\n\t" \
"buffer_load_dwordx2 v[128:129], v220, s[24:27], s45 offen offset:512 sc1\n\t" \
"buffer_load_dwordx2 v[130:131], v220, s[24:27], s46 offen offset:512 sc1\n\t" \
"buffer_load_dwordx2 v[132:133], v220, s[24:27], s47 offen offset:512 sc1\n\t" \
"buffer_load_dwordx2 v[136:137], v220, s[24:27], s45 offen offset:1024 sc1\n\t" \
"buffer_load_dwordx2 v[138:139], v220, s[24:27], s46 offen offset:1024 sc1\n\t" \
"buffer_load_dwordx2 v[140:141], v220, s[24:27], s47 offen offset:1024 sc1\n\t" \
"buffer_load_dwordx2 v[144:145], v220, s[24:27], s45 offen offset:1536 sc1\n\t" \
"buffer_load_dwordx2 v[146:147], v220, s[24:27], s46 offen offset:1536 sc1\n\t" \
"buffer_load_dwordx2 v[148:149], v220, s[24:27], s47 offen offset:1536 sc1\n\t" \
"buffer_load_dwordx2 v[152:153], v220, s[24:27], s45 offen offset:2048 sc1\n\t" \
"buffer_load_dwordx2 v[154:155], v220, s[24:27], s46 offen offset:2048 sc1\n\t" \
"buffer_load_dwordx2 v[156:157], v220, s[24:27], s47 offen offset:2048 sc1\n\t" \
"buffer_load_dwordx2 v[160:161], v220, s[24:27], s45 offen offset:2560 sc1\n\t" \
"buffer_load_dwordx2 v[162:163], v220, s[24:27], s46 offen offset:2560 sc1\n\t" \
"buffer_load_dwordx2 v[164:165], v220, s[24:27], s47 offen offset:2560 sc1\n\t" \
"buffer_load_dwordx2 v[168:169], v220, s[24:27], s45 offen offset:3072 sc1\n\t" \
"buffer_load_dwordx2 v[170:171], v220, s[24:27], s46 offen offset:3072 sc1\n\t" \
"buffer_load_dwordx2 v[172:173], v220, s[24:27], s47 offen offset:3072 sc1\n\t" \
"s_waitcnt vmcnt(18)\n\t" \
"v_mfma_f32_16x16x32_bf16 v[8:11], v[208:211], v[120:123], v[8:11]\n\t" \
"v_mfma_f32_16x16x32_bf16 v[8:11], v[208:211], v[124:127], v[8:11]\n\t" \
"s_waitcnt vmcnt(15)\n\t" \
"v_mfma_f32_16x16x32_bf16 v[12:15], v[208:211], v[128:131], v[12:15]\n\t" \
"v_mfma_f32_16x16x32_bf16 v[12:15], v[208:211], v[132:135], v[12:15]\n\t" \
"s_waitcnt vmcnt(12)\n\t" \
"v_mfma_f32_16x16x32_bf16 v[16:19], v[208:211], v[136:139], v[16:19]\n\t" \
"v_mfma_f32_16x16x32_bf16 v[16:19], v[208:211], v[140:143], v[16:19]\n\t" \
"s_nop 1\n\t" \
"v_cvt_pk_bf16_f32 v8, v8, v9\n\t" \
"v_cvt_pk_bf16_f32 v9, v10, v11\n\t" \
"buffer_store_dwordx2 v[8:9], v1, s[28:31], s70 offen sc1\n\t" \
"s_add_u32 s70, s70, 0x10000\n\t" \
"s_waitcnt vmcnt(10)\n\t" \
"v_mfma_f32_16x16x32_bf16 v[20:23], v[208:211], v[144:147], v[20:23]\n\t" \
"v_mfma_f32_16x16x32_bf16 v[20:23], v[208:211], v[148:151], v[20:23]\n\t" \
"v_cvt_pk_bf16_f32 v12, v12, v13\n\t" \
"v_cvt_pk_bf16_f32 v13, v14, v15\n\t" \
"buffer_store_dwordx2 v[12:13], v1, s[28:31], s70 offen sc1\n\t" \
"s_add_u32 s70, s70, 0x10000\n\t" \
"s_waitcnt vmcnt(8)\n\t" \
"v_mfma_f32_16x16x32_bf16 v[24:27], v[208:211], v[152:155], v[24:27]\n\t" \
"v_mfma_f32_16x16x32_bf16 v[24:27], v[208:211], v[156:159], v[24:27]\n\t" \
"v_cvt_pk_bf16_f32 v16, v16, v17\n\t" \
"v_cvt_pk_bf16_f32 v17, v18, v19\n\t" \
"buffer_store_dwordx2 v[16:17], v1, s[28:31], s70 offen sc1\n\t" \
"s_add_u32 s70, s70, 0x10000\n\t" \
"s_waitcnt vmcnt(6)\n\t" \
"v_mfma_f32_16x16x32_bf16 v[28:31], v[208:211], v[160:163], v[28:31]\n\t" \
"v_mfma_f32_16x16x32_bf16 v[28:31], v[208:211], v[164:167], v[28:31]\n\t" \
"v_cvt_pk_bf16_f32 v20, v20, v21\n\t" \
"v_cvt_pk_bf16_f32 v21, v22, v23\n\t" \
"buffer_store_dwordx2 v[20:21], v1, s[28:31], s70 offen sc1\n\t" \
"s_add_u32 s70, s70, 0x10000\n\t" \
"s_waitcnt vmcnt(4)\n\t" \
"v_mfma_f32_16x16x32_bf16 v[32:35], v[208:211], v[168:171], v[32:35]\n\t" \
"v_mfma_f32_16x16x32_bf16 v[32:35], v[208:211], v[172:175], v[32:35]\n\t" \
"v_cvt_pk_bf16_f32 v24, v24, v25\n\t" \
"v_cvt_pk_bf16_f32 v25, v26, v27\n\t" \
"buffer_store_dwordx2 v[24:25], v1, s[28:31], s70 offen sc1\n\t" \
"s_add_u32 s70, s70, 0x10000\n\t" \
"v_cvt_pk_bf16_f32 v28, v28, v29\n\t" \
"v_cvt_pk_bf16_f32 v29, v30, v31\n\t" \
"buffer_store_dwordx2 v[28:29], v1, s[28:31], s70 offen sc1\n\t" \
"s_add_u32 s70, s70, 0x10000\n\t" \
"v_cvt_pk_bf16_f32 v32, v32, v33\n\t" \
"v_cvt_pk_bf16_f32 v33, v34, v35\n\t" \
"buffer_store_dwordx2 v[32:33], v1, s[28:31], s70 offen sc1\n\t" \
"s_add_u32 s70, s70, 0x10000\n\t" \
"s_cmp_eq_u32 s32, 0\n\t" \
"s_cbranch_scc0 L_gdone_%=\n\t" \
"v_mov_b32 v2, s50\n\t" \
"v_mov_b32 v3, s51\n\t" \
"s_mov_b64 exec, 1\n\t" \
"buffer_store_dwordx2 v[2:3], off, s[24:27], s83 offset:0 sc1\n\t" \
"s_mov_b64 exec, -1\n\t" \
"L_gdone_%=:\n\t" \
"s_add_u32 s32, s32, 4\n\t" \
"s_branch L_end_%=\n\t" \
"L_os59_%=:\n\t" \
"s_mov_b32 s3, 200000\n\t" \
"L_sl62_%=:\n\t" \
"ds_read_b128 v[246:249], v229 offset:0\n\t" \
"s_sub_u32 s3, s3, 1\n\t" \
"s_cbranch_scc1 L_sd62_%=\n\t" \
"s_waitcnt lgkmcnt(0)\n\t" \
"v_min_u32 v246, v247, v249\n\t" \
"v_cmp_gt_u32 vcc, s68, v246\n\t" \
"s_cbranch_vccnz L_sl62_%=\n\t" \
"L_sd62_%=:\n\t" \
"s_waitcnt lgkmcnt(0)\n\t" \
"s_branch L_ob59_%=\n\t" \
"L_os60_%=:\n\t" \
"s_mov_b32 s3, 200000\n\t" \
"L_sl63_%=:\n\t" \
"ds_read_b128 v[246:249], v229 offset:0\n\t" \
"s_sub_u32 s3, s3, 1\n\t" \
"s_cbranch_scc1 L_sd63_%=\n\t" \
"s_waitcnt lgkmcnt(0)\n\t" \
"v_min_u32 v246, v246, v248\n\t" \
"v_cmp_gt_u32 vcc, s68, v246\n\t" \
"s_cbranch_vccnz L_sl63_%=\n\t" \
"L_sd63_%=:\n\t" \
"s_waitcnt lgkmcnt(0)\n\t" \
"s_branch L_ob60_%=\n\t" \
"L_end_%=:\n\t" \
"s_endpgm\n\t"
#define KERNEL_CLOBBERS "v1", "v2", "v3", "v4", "v5", "v6", "v7", "v8", "v9", "v10", "v11", "v12", "v13", "v14", "v15", "v16", "v17", "v18", "v19", "v20", "v21", "v22", "v23", "v24", "v25", "v26", "v27", "v28", "v29", "v30", "v31", "v32", "v33", "v34", "v35", "v36", "v37", "v38", "v39", "v40", "v41", "v42", "v43", "v44", "v45", "v46", "v47", "v48", "v49", "v50", "v51", "v52", "v53", "v54", "v55", "v56", "v57", "v58", "v59", "v60", "v61", "v62", "v63", "v64", "v65", "v66", "v67", "v68", "v69", "v70", "v71", "v72", "v73", "v74", "v75", "v76", "v77", "v78", "v79", "v80", "v81", "v82", "v83", "v84", "v85", "v86", "v87", "v88", "v89", "v90", "v91", "v92", "v93", "v94", "v95", "v96", "v97", "v98", "v99", "v100", "v101", "v102", "v103", "v104", "v105", "v106", "v107", "v108", "v109", "v110", "v111", "v112", "v113", "v114", "v115", "v116", "v117", "v118", "v119", "v120", "v121", "v122", "v123", "v124", "v125", "v126", "v127", "v128", "v129", "v130", "v131", "v132", "v133", "v134", "v135", "v136", "v137", "v138", "v139", "v140", "v141", "v142", "v143", "v144", "v145", "v146", "v147", "v148", "v149", "v150", "v151", "v152", "v153", "v154", "v155", "v156", "v157", "v158", "v159", "v160", "v161", "v162", "v163", "v164", "v165", "v166", "v167", "v168", "v169", "v170", "v171", "v172", "v173", "v174", "v175", "v176", "v177", "v178", "v179", "v180", "v181", "v182", "v183", "v184", "v185", "v186", "v187", "v188", "v189", "v190", "v191", "v192", "v193", "v194", "v195", "v196", "v197", "v198", "v199", "v200", "v201", "v202", "v203", "v204", "v205", "v206", "v207", "v208", "v209", "v210", "v211", "v212", "v213", "v214", "v215", "v216", "v217", "v218", "v219", "v220", "v221", "v222", "v223", "v224", "v225", "v226", "v227", "v228", "v229", "v230", "v231", "v232", "v233", "v234", "v235", "v236", "v237", "v238", "v239", "v240", "v241", "v242", "v243", "v244", "v245", "v246", "v247", "v248", "v249", "v250", "v251", "v252", "v253", "v254", "v255", "s3", "s4", "s5", "s6", "s7", "s8", "s9", "s10", "s11", "s12", "s13", "s14", "s15", "s16", "s17", "s18", "s19", "s20", "s21", "s22", "s23", "s24", "s25", "s26", "s27", "s28", "s29", "s30", "s31", "s32", "s33", "s34", "s35", "s36", "s37", "s38", "s39", "s40", "s41", "s42", "s43", "s44", "s45", "s46", "s47", "s48", "s49", "s50", "s51", "s52", "s53", "s54", "s55", "s56", "s57", "s58", "s59", "s60", "s61", "s62", "s63", "s64", "s65", "s66", "s67", "s68", "s69", "s70", "s71", "s72", "s73", "s74", "s75", "s76", "s77", "s78", "s79", "s80", "s81", "s82", "s83", "s84", "s85", "s86", "s87", "s88", "s89", "s90", "s91", "s92", "s93", "s94", "s95", "s96", "s97", "s98", "s99", "vcc", "scc", "memory"
__global__ __launch_bounds__(512, 2) void fused_kernel(const void* xA, const void* kA, const void* vA, const void* wqA, const void* woA,
                                                       const void* xB, const void* kB, const void* vB, const void* wqB, const void* woB, void* out, void* ws) {
  unsigned tid = threadIdx.x;
  asm volatile(KERNEL_ASM : "+{v0}"(tid) : "{s[0:1]}"(__builtin_amdgcn_kernarg_segment_ptr()), "{s2}"(blockIdx.x) : KERNEL_CLOBBERS);
}
extern "C" void kernel_launch(void* const* d_in, const int* in_sizes, int n_in, void* d_out, int out_size, void* d_ws, size_t ws_size, hipStream_t stream) {
  (void)hipFuncSetAttribute((const void*)fused_kernel, hipFuncAttributeMaxDynamicSharedMemorySize, 163840);
  fused_kernel<<<256, 512, 163840, stream>>>(d_in[0], d_in[1], d_in[2], d_in[3], d_in[4], d_in[5], d_in[6], d_in[7], d_in[8], d_in[9], d_out, d_ws);
}
```
